```python
import torch

def _softmax(x, axis=-1):
    return torch.softmax(x, dim=axis)
def _gelu(x):
    return torch.nn.functional.gelu(x, approximate="tanh")
def _silu(x):
    return torch.nn.functional.silu(x)
def _sigmoid(x):
    return torch.sigmoid(x)
def _layernorm(x, axis=-1, eps=1e-5):
    m = x.mean(dim=axis, keepdim=True)
    v = x.var(dim=axis, keepdim=True, unbiased=False)
    return (x - m) / torch.sqrt(v + eps)
def _scatter_add(tgt, idx, src, axis=0):
    return tgt.clone().index_add_(axis, idx, src)
def _segment_sum(src, seg, n):
    out = torch.zeros((n,) + src.shape[1:], dtype=src.dtype)
    return out.index_add_(0, seg, src)
def _pad(x, pw):
    flat = tuple(v for pair in reversed(pw) for v in pair)
    return torch.nn.functional.pad(x, flat)

kernel_name = 'concat_heads__spectral_gfnet__spectral_gfnet__prompt'

def setup_inputs():

    _w = (lambda a: (lambda t: t.to(torch.bfloat16) if t.is_floating_point() else t)(torch.as_tensor(a)))
    def _A_setup():
        B, S, D = 1, 2048, 1024
        Df = D // 2 + 1
        return {
            "x": _w((lambda *s: torch.randn(*s, dtype=torch.bfloat16) * 0.05)(B, S, D)),
            "w_r": _w((lambda *s: torch.randn(*s, dtype=torch.bfloat16) * 0.05)(S, Df) * 0.02),
            "w_i": _w((lambda *s: torch.randn(*s, dtype=torch.bfloat16) * 0.05)(S, Df) * 0.02),
        }
    _A_in = _A_setup()

    def _B_setup():
        B, S, D = 1, 2048, 1024
        Df = D // 2 + 1
        return {
            "x": _w((lambda *s: torch.randn(*s, dtype=torch.bfloat16) * 0.05)(B, S, D)),
            "w_r": _w((lambda *s: torch.randn(*s, dtype=torch.bfloat16) * 0.05)(S, Df) * 0.02),
            "w_i": _w((lambda *s: torch.randn(*s, dtype=torch.bfloat16) * 0.05)(S, Df) * 0.02),
        }
    _B_in = _B_setup()

    return {
        "A_x": _A_in["x"],
        "A_w_r": _A_in["w_r"],
        "A_w_i": _A_in["w_i"],
        "B_x": _B_in["x"],
        "B_w_r": _B_in["w_r"],
        "B_w_i": _B_in["w_i"],
    }

def reference(A_x, A_w_r, A_w_i, B_x, B_w_r, B_w_i):
    def _A_ref(x, w_r, w_i):
        B, S, D = x.shape
        X = (lambda x, axes=(-2,-1): torch.fft.rfft2(x.float(), dim=axes))(x, axes=(-2, -1))
        W = w_r + 1j * w_i
        X = X * W
        y = (lambda x, s, axes=(-2,-1): torch.fft.irfft2(x, s=s, dim=axes).to(torch.bfloat16))(X, (S, D), axes=(-2, -1))
        return y

    def _B_ref(x, w_r, w_i):
        B, S, D = x.shape
        X = (lambda x, axes=(-2,-1): torch.fft.rfft2(x.float(), dim=axes))(x, axes=(-2, -1))
        W = w_r + 1j * w_i
        X = X * W
        y = (lambda x, s, axes=(-2,-1): torch.fft.irfft2(x, s=s, dim=axes).to(torch.bfloat16))(X, (S, D), axes=(-2, -1))
        return y

    a = _A_ref(A_x, A_w_r, A_w_i)
    b = _B_ref(B_x, B_w_r, B_w_i)
    return torch.concatenate([a, b], axis=-1)
```

```cpp
#include <hip/hip_runtime.h>

struct Params { const void *ws, *x0, *y, *x1, *wr0, *wr1, *wi0, *wi1; };

__global__ __attribute__((aligned(4096))) __launch_bounds__(512, 2) void gfnet_fused(Params P) {
  __shared__ __attribute__((aligned(16))) unsigned char lds[118784];
  asm volatile(
    "s_load_dwordx16 s[12:27], s[0:1], 0\n"
    "v_lshrrev_b32 v254, 6, v0\n"
    "s_and_b32 s32, s2, 0x7f\n"
    "v_readfirstlane_b32 s30, v254\n"
    "s_getpc_b64 s[28:29]\n"
    ".Lpc_%=:\n"
    "v_lshlrev_b32 v254, 5, v0\n"
    "v_min_u32 v254, .Lend_%= - .Lpc_%= - 4, v254\n"
    "global_load_dword v255, v254, s[28:29]\n"
    "v_bfe_u32 v3, v0, 4, 2\n"
    "v_lshlrev_b32 v140, 4, v0\n"
    "v_and_b32 v141, 8, v0\n"
    "v_and_b32 v140, 0x70, v140\n"
    "v_lshl_or_b32 v140, v3, 20, v140\n"
    "v_lshl_or_b32 v140, v141, 11, v140\n"
    "s_lshl_b32 s44, s30, 8\n"
    "s_or_b32 s44, s44, s32\n"
    "s_lshl_b32 s40, s44, 7\n"
    "s_add_u32 s41, s40, 0x40000\n"
    "s_add_u32 s42, s40, 0x80000\n"
    "s_add_u32 s43, s40, 0xc0000\n"
    "s_and_b32 s44, s32, 7\n"
    "s_lshr_b32 s36, s32, 3\n"
    "s_lshl4_add_u32 s36, s44, s36\n"
    "s_lshl3_add_u32 s36, s36, s30\n"
    "s_lshl_b32 s36, s36, 13\n"
    "s_add_u32 s45, s36, 0x1000000\n"
    "s_mov_b32 s6, 0x400000\n"
    "s_mov_b32 s7, 0x20000\n"
    "s_mov_b32 s10, 0x1800000\n"
    "s_mov_b32 s11, s7\n"
    "s_bfe_u32 s3, s2, 65543\n"
    "s_lshl_b32 s8, s3, 23\n"
    "s_add_u32 s8, s8, 0x200000\n"
    "s_cmp_lg_u32 s3, 0\n"
    "s_waitcnt lgkmcnt(0)\n"
    "s_cselect_b64 s[4:5], s[18:19], s[14:15]\n"
    "s_add_u32 s8, s8, s12\n"
    "s_addc_u32 s9, s13, 0\n"
    "s_cmp_lg_u32 s30, 0\n"
    "s_cbranch_scc1 .Lnpa_%=\n"
    "s_buffer_load_dword s62, s[8:11], s45 offset:0\n"
    "s_buffer_load_dword s63, s[8:11], s45 offset:8192\n"
    ".Lnpa_%=:\n"
    "buffer_load_dwordx4 v[192:195], v140, s[4:7], s40 offen nt\n"
    "buffer_load_dwordx4 v[196:199], v140, s[4:7], s41 offen nt\n"
    "buffer_load_dwordx4 v[200:203], v140, s[4:7], s42 offen nt\n"
    "buffer_load_dwordx4 v[204:207], v140, s[4:7], s43 offen nt\n"
    "s_cmp_lg_u32 s3, 0\n"
    "s_cselect_b64 s[20:21], s[22:23], s[20:21]\n"
    "s_cselect_b64 s[24:25], s[26:27], s[24:25]\n"
    "s_lshl_b32 s14, s3, 18\n"
    "s_add_u32 s12, s12, s14\n"
    "s_addc_u32 s13, s13, 0\n"
    "s_mov_b32 s14, 0x40000\n"
    "s_mov_b32 s15, s7\n"
    "s_lshl_b32 s3, s3, 11\n"
    "s_add_u32 s16, s16, s3\n"
    "s_addc_u32 s17, s17, 0\n"
    "s_sub_u32 s18, 0x800000, s3\n"
    "s_mov_b32 s19, s7\n"
    "s_mov_b32 s22, 2101248\n"
    "s_mov_b32 s23, s7\n"
    "s_mov_b64 s[26:27], s[22:23]\n"
    "s_lshr_b32 s69, s32, 3\n"
    "s_and_b32 s44, s69, 0x18\n"
    "s_add_u32 s69, s69, s44\n"
    "s_and_b32 s44, s32, 1\n"
    "s_lshl3_add_u32 s69, s44, s69\n"
    "s_lshl3_add_u32 s69, s69, s30\n"
    "s_lshl_b32 s69, s69, 7\n"
    "s_add_u32 s69, s69, 16384\n"
    "v_mov_b32 v244, 0\n"
    "s_add_u32 s44, s69, 32768\n"
    "s_xor_b32 s45, s32, s30\n"
    "s_and_b32 s45, s45, 6\n"
    "s_cselect_b32 s44, 0, s44\n"
    "s_cselect_b32 s45, 0, s69\n"
    "v_writelane_b32 v244, s45, 1\n"
    "v_writelane_b32 v244, s44, 2\n"
    "buffer_load_dwordx2 v[142:143], v244, s[12:15], 0 offen sc1\n"
    "v_and_b32 v1, 63, v0\n"
    "v_and_b32 v2, 15, v0\n"
    "s_lshr_b32 s44, s32, 4\n"
    "s_and_b32 s45, s32, 15\n"
    "s_lshl4_add_u32 s33, s45, s44\n"
    "s_lshr_b32 s44, s32, 3\n"
    "s_cmp_eq_u32 s44, 1\n"
    "s_cselect_b32 s44, 120, 0\n"
    "s_sub_u32 s33, s33, s44\n"
    "s_lshl_b32 s34, s30, 8\n"
    "s_add_u32 s34, s34, s33\n"
    "s_cmp_ge_u32 s30, 4\n"
    "s_cselect_b32 s44, 896, 0\n"
    "s_cmp_eq_u32 s32, 0\n"
    "s_cselect_b32 s44, s44, 0\n"
    "s_sub_u32 s34, s34, s44\n"
    "s_sub_u32 s35, 0, s34\n"
    "s_and_b32 s35, s35, 0x7ff\n"
    "s_mul_i32 s60, s34, 1026\n"
    "s_mul_i32 s61, s35, 1026\n"
    "s_add_u32 s34, s60, 2\n"
    "s_and_b32 s34, s34, -4\n"
    "s_add_u32 s35, s61, 2\n"
    "s_and_b32 s35, s35, -4\n"
    "s_bfe_u32 s31, s30, 0x10001\n"
    "s_lshl_b32 s29, s30, 12\n"
    "s_and_b32 s44, s29, 0x1000\n"
    "s_sub_u32 s36, s36, s44\n"
    "s_lshl_b32 s67, s30, 12\n"
    "s_add_u32 s67, s67, 16384\n"
    "s_add_u32 s29, s29, 65536\n"
    "s_and_b32 s44, s32, 7\n"
    "s_lshl_b32 s57, 1, s44\n"
    "v_lshlrev_b32 v15, 4, v1\n"
    "s_mov_b32 s58, 0x05040100\n"
    "s_mov_b32 s59, 0x07060302\n"
    "s_mov_b32 s56, 0x2000\n"
    "s_or_b32 s37, s32, s30\n"
    "s_cbranch_scc1 .Lnsw_%=\n"
    "s_mov_b32 s44, 1050624\n"
    "v_mul_u32_u24 v153, s44, v1\n"
    "s_mov_b32 m0, 98304\n"
    "s_nop 0\n"
    "buffer_load_dwordx4 v15, s[20:23], 0 offen lds\n"
    "s_mov_b32 m0, 99328\n"
    "s_nop 0\n"
    "buffer_load_dwordx4 v15, s[24:27], 0 offen lds\n"
    "s_mov_b32 m0, 100352\n"
    "s_nop 0\n"
    "buffer_load_dwordx4 v15, s[20:23], s44 offen lds\n"
    "s_mov_b32 m0, 101376\n"
    "s_nop 0\n"
    "buffer_load_dwordx4 v15, s[24:27], s44 offen lds\n"
    "buffer_load_ushort v153, v153, s[20:23], 0 offen offset:1024\n"
    ".Lnsw_%=:\n"
    "s_waitcnt lgkmcnt(1)\n"
    "s_nop 1\n"
    "s_getreg_b32 s68, hwreg(7, 8, 4)\n"
    "v_lshrrev_b32 v160, 1, v2\n"
    "v_and_b32 v161, 1, v0\n"
    "v_lshlrev_b32 v250, 2, v3\n"
    "v_cmp_gt_u32 vcc, 2, v2\n"
    "v_cvt_f32_u32 v160, v160\n"
    "v_cvt_f32_u32 v161, v161\n"
    "v_cvt_f32_u32 v162, v250\n"
    "v_cvt_f32_u32 v178, s30\n"
    "v_mul_f32 v160, 0x3d800000, v160\n"
    "v_mul_f32 v170, 0.5, v161\n"
    "v_mul_f32 v161, 0x3e800000, v161\n"
    "v_cndmask_b32 v163, v160, v170, vcc\n"
    "v_cndmask_b32 v164, v161, 0, vcc\n"
    "s_cmp_eq_u32 s30, 0\n"
    "s_cselect_b64 s[50:51], vcc, 0\n"
    "s_cselect_b32 s44, 0xffff, -1\n"
    "s_cselect_b32 s45, 0xffff0000, -1\n"
    "s_cselect_b32 s46, 0x3d000000, 0.5\n"
    "s_cselect_b32 s47, 0x3e800000, 0\n"
    "v_fma_f32 v165, v162, v163, v164\n"
    "v_add_f32 v166, v165, v163\n"
    "v_add_f32 v167, v166, v163\n"
    "v_add_f32 v168, v167, v163\n"
    "v_cos_f32 v170, v165\n"
    "v_cos_f32 v171, v166\n"
    "v_cos_f32 v172, v167\n"
    "v_cos_f32 v173, v168\n"
    "v_fmac_f32 v160, 0x3b800000, v178\n"
    "v_cvt_pk_bf16_f32 v128, v170, v171\n"
    "v_cvt_pk_bf16_f32 v129, v172, v173\n"
    "v_cndmask_b32 v163, v160, v163, s[50:51]\n"
    "v_cndmask_b32 v164, v161, v164, s[50:51]\n"
    "v_fma_f32 v165, v162, v163, v164\n"
    "v_add_f32 v166, v165, v163\n"
    "v_add_f32 v167, v166, v163\n"
    "v_add_f32 v168, v167, v163\n"
    "v_cos_f32 v170, v165\n"
    "v_sin_f32 v174, v165\n"
    "v_cos_f32 v171, v166\n"
    "v_sin_f32 v175, v166\n"
    "v_cos_f32 v172, v167\n"
    "v_sin_f32 v176, v167\n"
    "v_cos_f32 v173, v168\n"
    "v_sin_f32 v177, v168\n"
    "v_cvt_pk_bf16_f32 v132, v170, v174\n"
    "v_cvt_pk_bf16_f32 v133, v171, v175\n"
    "v_cvt_pk_bf16_f32 v134, v172, v176\n"
    "v_cvt_pk_bf16_f32 v135, v173, v177\n"
    "v_and_b32 v132, s44, v132\n"
    "v_and_b32 v133, s44, v133\n"
    "v_and_b32 v134, s44, v134\n"
    "v_and_b32 v135, s44, v135\n"
    "v_add_f32 v163, s46, v160\n"
    "v_add_f32 v164, s47, v161\n"
    "v_fma_f32 v165, v162, v163, v164\n"
    "v_add_f32 v166, v165, v163\n"
    "v_add_f32 v167, v166, v163\n"
    "v_add_f32 v168, v167, v163\n"
    "v_cos_f32 v170, v165\n"
    "v_sin_f32 v174, v165\n"
    "v_cos_f32 v171, v166\n"
    "v_sin_f32 v175, v166\n"
    "v_cos_f32 v172, v167\n"
    "v_sin_f32 v176, v167\n"
    "v_cos_f32 v173, v168\n"
    "v_sin_f32 v177, v168\n"
    "v_cvt_pk_bf16_f32 v136, v170, v174\n"
    "v_cvt_pk_bf16_f32 v137, v171, v175\n"
    "v_cvt_pk_bf16_f32 v138, v172, v176\n"
    "v_cvt_pk_bf16_f32 v139, v173, v177\n"
    "v_and_b32 v136, s45, v136\n"
    "v_and_b32 v137, s45, v137\n"
    "v_and_b32 v138, s45, v138\n"
    "v_and_b32 v139, s45, v139\n"
    "s_lshl_b32 s44, s30, 9\n"
    "s_lshl_b32 s45, s31, 6\n"
    "s_or_b32 s44, s44, s45\n"
    "v_and_b32 v144, 0x70, v140\n"
    "v_mul_u32_u24 v145, 36, v141\n"
    "v_lshl_or_b32 v144, v3, 13, v144\n"
    "v_bitop3_b32 v144, v144, v145, s44 bitop3:0x96\n"
    "s_lshl_b32 s44, s30, 12\n"
    "v_and_b32 v146, 3, v0\n"
    "v_lshl_or_b32 v146, v146, 3, s44\n"
    "v_lshl_or_b32 v146, v3, 10, v146\n"
    "v_and_b32 v147, 12, v0\n"
    "v_lshl_or_b32 v146, v147, 5, v146\n"
    "v_and_b32 v147, 24, v0\n"
    "v_lshl_or_b32 v146, v147, 2, v146\n"
    "v_xor_b32 v147, 32, v146\n"
    "v_xor_b32 v148, 64, v146\n"
    "v_xor_b32 v149, 0x60, v146\n"
    "v_lshlrev_b32 v150, 4, v2\n"
    "v_lshlrev_b32 v251, 1, v3\n"
    "v_lshl_or_b32 v13, v3, 18, v150\n"
    "v_lshl_or_b32 v14, v3, 17, v150\n"
    "v_lshl_or_b32 v150, v3, 9, v150\n"
    "s_lshl_b32 s45, s30, 2\n"
    "v_mov_b32 v160, s68\n"
    "v_mov_b32 v161, s45\n"
    "v_mov_b32 v162, 0\n"
    "ds_write_b32 v161, v160 offset:61440\n"
    "s_waitcnt lgkmcnt(0)\n"
    "s_barrier\n"
    "ds_read_b32 v164, v162 offset:61440\n"
    "s_getreg_b32 s44, hwreg(20, 0, 3)\n"
    "s_sub_u32 s45, s44, s2\n"
    "s_and_b32 s45, s45, 7\n"
    "s_lshr_b32 s3, 0xf0f0, s45\n"
    "s_bitcmp1_b32 s44, 2\n"
    "s_cselect_b32 s45, -1, 0\n"
    "s_xor_b32 s3, s3, s45\n"
    "s_bfe_u32 s45, s32, 65540\n"
    "s_lshl_b32 s45, 0x55, s45\n"
    "s_and_b32 s3, s3, s45\n"
    "s_add_u32 s44, s8, s36\n"
    "s_xor_b32 s44, s44, s12\n"
    "s_lshr_b32 s44, s44, 8\n"
    "s_andn2_b32 s69, s69, s56\n"
    "s_waitcnt lgkmcnt(0)\n"
    "v_cmp_ge_i32 vcc, 0, v164\n"
    "s_nop 0\n"
    "s_xor_b32 s3, s3, vcc_lo\n"
    "s_and_b32 s3, s3, 0xff\n"
    "s_xor_b32 s44, s44, vcc_lo\n"
    "s_and_b32 s44, s44, s56\n"
    "s_or_b32 s69, s69, s44\n"
    "s_and_b32 s66, s32, 0x38\n"
    "s_lshl_b32 s66, s66, 5\n"
    "s_lshl_b32 s44, s3, 13\n"
    "v_lshrrev_b32 v151, v251, s44\n"
    "v_and_or_b32 v151, v151, s56, v150\n"
    "s_xor_b32 s44, s66, 0\n"
    "v_xor_b32 v151, s44, v151\n"
    "s_lshl_b32 s44, s3, 12\n"
    "v_lshrrev_b32 v152, v251, s44\n"
    "v_and_or_b32 v152, v152, s56, v150\n"
    "s_xor_b32 s44, s66, 256\n"
    "v_xor_b32 v152, s44, v152\n"
    "s_waitcnt vmcnt(3)\n"
    "v_perm_b32 v208, v196, v192, s58\n"
    "v_perm_b32 v210, v196, v192, s59\n"
    "v_perm_b32 v212, v197, v193, s58\n"
    "v_perm_b32 v214, v197, v193, s59\n"
    "v_perm_b32 v216, v198, v194, s58\n"
    "v_perm_b32 v218, v198, v194, s59\n"
    "v_perm_b32 v220, v199, v195, s58\n"
    "v_perm_b32 v222, v199, v195, s59\n"
    "s_waitcnt vmcnt(1)\n"
    "v_perm_b32 v209, v204, v200, s58\n"
    "v_perm_b32 v211, v204, v200, s59\n"
    "v_perm_b32 v213, v205, v201, s58\n"
    "v_mfma_f32_16x16x16_bf16 v[160:163], v[128:129], v[208:209], 0\n"
    "v_perm_b32 v215, v205, v201, s59\n"
    "v_mfma_f32_16x16x16_bf16 v[164:167], v[128:129], v[210:211], 0\n"
    "v_perm_b32 v217, v206, v202, s58\n"
    "v_mfma_f32_16x16x16_bf16 v[168:171], v[128:129], v[212:213], 0\n"
    "v_perm_b32 v219, v206, v202, s59\n"
    "v_mfma_f32_16x16x16_bf16 v[172:175], v[128:129], v[214:215], 0\n"
    "v_perm_b32 v221, v207, v203, s58\n"
    "v_mfma_f32_16x16x16_bf16 v[176:179], v[128:129], v[216:217], 0\n"
    "v_perm_b32 v223, v207, v203, s59\n"
    "v_mfma_f32_16x16x16_bf16 v[180:183], v[128:129], v[218:219], 0\n"
    "v_cvt_pk_bf16_f32 v224, v160, v164\n"
    "v_cvt_pk_bf16_f32 v228, v161, v165\n"
    "v_mfma_f32_16x16x16_bf16 v[184:187], v[128:129], v[220:221], 0\n"
    "v_cvt_pk_bf16_f32 v232, v162, v166\n"
    "v_cvt_pk_bf16_f32 v236, v163, v167\n"
    "v_mfma_f32_16x16x16_bf16 v[188:191], v[128:129], v[222:223], 0\n"
    "v_cvt_pk_bf16_f32 v225, v168, v172\n"
    "v_cvt_pk_bf16_f32 v229, v169, v173\n"
    "v_cvt_pk_bf16_f32 v233, v170, v174\n"
    "v_cvt_pk_bf16_f32 v237, v171, v175\n"
    "v_cvt_pk_bf16_f32 v226, v176, v180\n"
    "v_cvt_pk_bf16_f32 v230, v177, v181\n"
    "v_cvt_pk_bf16_f32 v234, v178, v182\n"
    "v_cvt_pk_bf16_f32 v238, v179, v183\n"
    "v_cvt_pk_bf16_f32 v227, v184, v188\n"
    "ds_write_b128 v144, v[224:227] offset:0\n"
    "v_cvt_pk_bf16_f32 v231, v185, v189\n"
    "ds_write_b128 v144, v[228:231] offset:128\n"
    "v_cvt_pk_bf16_f32 v235, v186, v190\n"
    "ds_write_b128 v144, v[232:235] offset:4096\n"
    "v_cvt_pk_bf16_f32 v239, v187, v191\n"
    "ds_write_b128 v144, v[236:239] offset:4224\n"
    "s_waitcnt lgkmcnt(0)\n"
    "s_barrier\n"
    "ds_read_b64_tr_b16 v[192:193], v146\n"
    "ds_read_b64_tr_b16 v[194:195], v146 offset:512\n"
    "ds_read_b64_tr_b16 v[196:197], v147\n"
    "ds_read_b64_tr_b16 v[198:199], v147 offset:512\n"
    "ds_read_b64_tr_b16 v[200:201], v148\n"
    "ds_read_b64_tr_b16 v[202:203], v148 offset:512\n"
    "ds_read_b64_tr_b16 v[204:205], v149\n"
    "ds_read_b64_tr_b16 v[206:207], v149 offset:512\n"
    "s_waitcnt lgkmcnt(6)\n"
    "v_mfma_f32_16x16x32_bf16 v[160:163], v[132:135], v[192:195], 0\n"
    "s_waitcnt lgkmcnt(4)\n"
    "v_mfma_f32_16x16x32_bf16 v[164:167], v[132:135], v[196:199], 0\n"
    "s_waitcnt lgkmcnt(2)\n"
    "v_mfma_f32_16x16x32_bf16 v[168:171], v[132:135], v[200:203], 0\n"
    "s_waitcnt lgkmcnt(0)\n"
    "v_mfma_f32_16x16x32_bf16 v[172:175], v[132:135], v[204:207], 0\n"
    "v_mfma_f32_16x16x32_bf16 v[176:179], v[136:139], v[192:195], 0\n"
    "s_nop 0\n"
    "v_cvt_pk_bf16_f32 v224, v160, v161\n"
    "s_nop 0\n"
    "v_cvt_pk_bf16_f32 v225, v164, v165\n"
    "v_mfma_f32_16x16x32_bf16 v[180:183], v[136:139], v[196:199], 0\n"
    "v_cvt_pk_bf16_f32 v226, v168, v169\n"
    "v_cvt_pk_bf16_f32 v228, v162, v163\n"
    "v_cvt_pk_bf16_f32 v229, v166, v167\n"
    "v_cvt_pk_bf16_f32 v227, v172, v173\n"
    "buffer_store_dwordx4 v[224:227], v151, s[8:11], s36 offen offset:0 sc1\n"
    "v_mfma_f32_16x16x32_bf16 v[184:187], v[136:139], v[200:203], 0\n"
    "v_cvt_pk_bf16_f32 v230, v170, v171\n"
    "v_cvt_pk_bf16_f32 v231, v174, v175\n"
    "v_mfma_f32_16x16x32_bf16 v[188:191], v[136:139], v[204:207], 0\n"
    "buffer_store_dwordx4 v[228:231], v152, s[8:11], s36 offen offset:0 sc1\n"
    "v_cvt_pk_bf16_f32 v232, v176, v177\n"
    "v_cvt_pk_bf16_f32 v233, v180, v181\n"
    "v_cvt_pk_bf16_f32 v236, v178, v179\n"
    "v_cvt_pk_bf16_f32 v237, v182, v183\n"
    "v_cvt_pk_bf16_f32 v234, v184, v185\n"
    "v_cvt_pk_bf16_f32 v238, v186, v187\n"
    "s_nop 0\n"
    "v_cvt_pk_bf16_f32 v235, v188, v189\n"
    "buffer_store_dwordx4 v[232:235], v151, s[8:11], s36 offen offset:2048 sc1\n"
    "v_cvt_pk_bf16_f32 v239, v190, v191\n"
    "buffer_store_dwordx4 v[236:239], v152, s[8:11], s36 offen offset:2048 sc1\n"
    "v_and_b32 v161, 1, v0\n"
    "v_lshrrev_b32 v4, 2, v2\n"
    "v_and_b32 v182, 3, v0\n"
    "v_lshl_or_b32 v4, v182, 2, v4\n"
    "v_cvt_f32_u32 v161, v161\n"
    "v_cvt_f32_u32 v179, v4\n"
    "v_cvt_f32_u32 v180, v2\n"
    "v_cvt_f32_u32 v181, v251\n"
    "v_mul_f32 v161, 0x3e800000, v161\n"
    "s_lshr_b32 s44, s30, 1\n"
    "s_cmp_eq_u32 s44, 1\n"
    "s_cselect_b32 s45, 0, 0x3d800000\n"
    "s_cselect_b32 s46, 0xbd000000, 0\n"
    "s_bitcmp1_b32 0xc2, s30\n"
    "s_cselect_b32 s47, 0x3d000000, 0\n"
    "v_mul_f32 v182, s45, v179\n"
    "v_fmac_f32 v182, s46, v180\n"
    "v_add_f32 v182, s47, v182\n"
    "s_cmp_eq_u32 s44, 0\n"
    "s_cselect_b32 s45, 2.0, 1.0\n"
    "s_cselect_b32 s46, 1.0, 0x41000000\n"
    "s_bitcmp1_b32 0xa8, s30\n"
    "s_cselect_b32 s47, 1.0, 0\n"
    "v_mul_f32 v183, s45, v181\n"
    "v_mul_f32 v163, s46, v182\n"
    "v_add_f32 v183, s47, v183\n"
    "s_lshl_b32 s44, s30, 11\n"
    "v_mul_f32 v165, v182, v183\n"
    "v_add_f32 v166, v165, v163\n"
    "v_add_f32 v167, v166, v163\n"
    "v_add_f32 v168, v167, v163\n"
    "v_cos_f32 v170, v165\n"
    "v_sin_f32 v174, v165\n"
    "v_cos_f32 v171, v166\n"
    "v_sin_f32 v175, v166\n"
    "v_cos_f32 v172, v167\n"
    "v_sin_f32 v176, v167\n"
    "v_cos_f32 v173, v168\n"
    "v_sin_f32 v177, v168\n"
    "v_add_u32 v182, s44, v15\n"
    "v_cvt_pk_bf16_f32 v184, v170, v174\n"
    "v_cvt_pk_bf16_f32 v185, v171, v175\n"
    "v_cvt_pk_bf16_f32 v186, v172, v176\n"
    "v_cvt_pk_bf16_f32 v187, v173, v177\n"
    "v_cvt_pk_bf16_f32 v188, -v174, v170\n"
    "v_cvt_pk_bf16_f32 v189, -v175, v171\n"
    "v_cvt_pk_bf16_f32 v190, -v176, v172\n"
    "v_cvt_pk_bf16_f32 v191, -v177, v173\n"
    "ds_write_b128 v182, v[184:187] offset:32768\n"
    "ds_write_b128 v182, v[188:191] offset:33792\n"
    "s_cmp_ge_u32 s30, 4\n"
    "s_cselect_b32 s45, 0x3a000000, 0\n"
    "s_cselect_b64 s[52:53], -1, 0\n"
    "s_cselect_b32 s49, 0x80000000, 0\n"
    "s_xor_b32 s49, s49, 0x3a000000\n"
    "s_and_b32 s48, s30, 1\n"
    "s_lshl4_add_u32 s46, s48, s31\n"
    "v_mul_f32 v182, 0x3a800000, v179\n"
    "v_cvt_f32_u32 v183, s46\n"
    "v_add_f32 v182, s45, v182\n"
    "v_add_f32 v183, v183, v181\n"
    "v_mul_f32 v165, v182, v183\n"
    "v_fmamk_f32 v166, v182, 0x41000000, v165\n"
    "v_cos_f32 v170, v165\n"
    "v_cos_f32 v171, v166\n"
    "v_sin_f32 v174, v165\n"
    "v_sin_f32 v175, v166\n"
    "v_rcp_f32 v172, v170\n"
    "v_rcp_f32 v173, v171\n"
    "s_lshr_b32 s44, s30, 1\n"
    "s_lshl_b32 s44, s44, 10\n"
    "s_lshl3_add_u32 s44, s48, s44\n"
    "v_add_u32 v163, s44, v15\n"
    "v_mul_f32 v170, -2.0, v170\n"
    "v_mul_f32 v171, -2.0, v171\n"
    "v_mul_f32 v172, v174, v172\n"
    "v_mul_f32 v173, v175, v173\n"
    "v_mul_f32 v170, v174, v170\n"
    "v_mul_f32 v171, v175, v171\n"
    "ds_write_b64 v163, v[172:173] offset:49152\n"
    "ds_write_b64 v163, v[170:171] offset:53248\n"
    "v_lshrrev_b32 v184, 2, v2\n"
    "v_lshl_add_u32 v184, s31, 2, v184\n"
    "v_add_u32 v185, s48, v251\n"
    "v_cndmask_b32 v186, v184, v185, s[52:53]\n"
    "v_cndmask_b32 v187, v185, v184, s[52:53]\n"
    "v_lshl_add_u32 v188, v186, 8, s33\n"
    "v_mov_b32 v189, -1\n"
    "s_mov_b64 s[54:55], 0\n"
    "s_cmp_lg_u32 s32, 0\n"
    "s_cbranch_scc1 .Lnsp1_%=\n"
    "v_cmp_le_u32 vcc, 4, v186\n"
    "v_and_b32 v182, 1, v0\n"
    "v_subrev_u32 v183, 896, v188\n"
    "v_cmp_eq_u32 s[54:55], 0, v186\n"
    "v_cndmask_b32 v188, v188, v183, vcc\n"
    "v_cndmask_b32 v183, 0, 1, vcc\n"
    "v_add_f32 v174, 0x3e800000, v161\n"
    "v_cndmask_b32 v161, v161, v174, vcc\n"
    "v_mov_b32 v174, 0xffff\n"
    "v_not_b32 v175, v174\n"
    "v_cndmask_b32 v189, v174, v175, vcc\n"
    "v_cmp_eq_u32 vcc, v182, v183\n"
    "v_lshlrev_b32 v174, 10, v182\n"
    "v_cndmask_b32 v188, v188, v174, s[54:55]\n"
    "v_cndmask_b32 v174, 0, -1, vcc\n"
    "v_cndmask_b32 v189, v189, v174, s[52:53]\n"
    "s_or_b64 vcc, s[52:53], s[54:55]\n"
    "s_and_b64 s[54:55], s[52:53], s[54:55]\n"
    "v_cndmask_b32 v161, v161, 0, vcc\n"
    ".Lnsp1_%=:\n"
    "v_mul_u32_u24 v188, v188, v187\n"
    "v_lshlrev_b32 v187, 31, v187\n"
    "v_cvt_f32_u32 v188, v188\n"
    "v_xor_b32 v187, 0x3f003f00, v187\n"
    "v_fmac_f32 v161, s49, v188\n"
    "v_bfe_i32 v182, v0, 1, 1\n"
    "v_cos_f32 v170, v161\n"
    "v_sin_f32 v174, v161\n"
    "v_and_b32 v182, 0x80008000, v182\n"
    "v_cvt_pk_bf16_f32 v170, v170, v174\n"
    "v_cndmask_b32 v170, v170, v187, s[54:55]\n"
    "v_and_b32 v170, v189, v170\n"
    "v_xor_b32 v171, v182, v170\n"
    "ds_write_b64 v163, v[170:171] offset:57344\n"
    "s_mov_b64 vcc, 6\n"
    "v_mov_b32 v255, s60\n"
    "v_mov_b32 v161, s61\n"
    "v_cndmask_b32 v255, v255, v161, vcc\n"
    "v_and_b32 v161, 2, v0\n"
    "v_lshl_add_u32 v255, v161, 9, v255\n"
    "s_waitcnt vmcnt(4)\n"
    "v_readfirstlane_b32 s38, v142\n"
    "v_readfirstlane_b32 s39, v143\n"
    "v_lshlrev_b32 v244, 3, v2\n"
    "v_lshlrev_b32 v245, 10, v1\n"
    "s_add_u32 s38, s38, 0x7f4a7c15\n"
    "s_addc_u32 s39, s39, 0x9e3779b9\n"
    "s_or_b32 s38, s38, 0xff\n"
    "s_xor_b32 s46, s38, s3\n"
    "v_mov_b32 v252, s46\n"
    "v_mov_b32 v253, s39\n"
    "s_and_b32 s44, s32, 7\n"
    "s_lshl_b32 s44, s44, 7\n"
    "s_and_b32 s45, s32, 0x78\n"
    "s_add_u32 s44, s44, s45\n"
    "s_add_u32 s44, s44, s67\n"
    "s_mov_b64 exec, 15\n"
    "s_waitcnt vmcnt(0) lgkmcnt(0)\n"
    "s_barrier\n"
    "buffer_store_dwordx2 v[252:253], v245, s[12:15], s44 offen sc1\n"
    "s_barrier\n"
    "buffer_load_ushort v161, v255, s[20:23], 0 offen nt\n"
    "buffer_load_ushort v162, v255, s[24:27], 0 offen nt\n"
    "s_mov_b64 exec, -1\n"
    "s_add_u32 m0, s29, 0\n"
    "s_nop 0\n"
    "buffer_load_dwordx4 v15, s[20:23], s34 offen nt lds\n"
    "s_add_u32 m0, s29, 1024\n"
    "s_nop 0\n"
    "buffer_load_dwordx4 v15, s[24:27], s34 offen nt lds\n"
    "s_mov_b64 exec, 0xffff\n"
    "v_mov_b32 v236, 0\n"
    "v_mov_b32 v237, 0\n"
    "buffer_atomic_or_x2 v[236:237], v244, s[12:15], s69 offen sc0\n"
    "s_mov_b64 exec, -1\n"
    "ds_read_b128 v[112:115], v15 offset:57344\n"
    "ds_read_b128 v[116:119], v15 offset:58368\n"
    "ds_read_b128 v[16:19], v15 offset:32768\n"
    "ds_read_b128 v[20:23], v15 offset:33792\n"
    "ds_read_b128 v[24:27], v15 offset:34816\n"
    "ds_read_b128 v[28:31], v15 offset:35840\n"
    "ds_read_b128 v[80:83], v15 offset:49152\n"
    "ds_read_b128 v[84:87], v15 offset:50176\n"
    "ds_read_b128 v[88:91], v15 offset:51200\n"
    "ds_read_b128 v[92:95], v15 offset:52224\n"
    "ds_read_b128 v[96:99], v15 offset:53248\n"
    "ds_read_b128 v[100:103], v15 offset:54272\n"
    "ds_read_b128 v[104:107], v15 offset:55296\n"
    "ds_read_b128 v[108:111], v15 offset:56320\n"
    "ds_read_b128 v[48:51], v15 offset:40960\n"
    "ds_read_b128 v[52:55], v15 offset:41984\n"
    "ds_read_b128 v[56:59], v15 offset:43008\n"
    "ds_read_b128 v[60:63], v15 offset:44032\n"
    "ds_read_b128 v[64:67], v15 offset:45056\n"
    "ds_read_b128 v[68:71], v15 offset:46080\n"
    "ds_read_b128 v[72:75], v15 offset:47104\n"
    "ds_read_b128 v[76:79], v15 offset:48128\n"
    "ds_read_b128 v[32:35], v15 offset:36864\n"
    "ds_read_b128 v[36:39], v15 offset:37888\n"
    "ds_read_b128 v[40:43], v15 offset:38912\n"
    "ds_read_b128 v[44:47], v15 offset:39936\n"
    "ds_read_b128 v[120:123], v15 offset:59392\n"
    "ds_read_b128 v[124:127], v15 offset:60416\n"
    "s_lshl_b32 s44, s30, 8\n"
    "s_lshl_b32 s45, s31, 7\n"
    "s_or_b32 s46, s44, s45\n"
    "v_and_b32 v192, 1, v0\n"
    "v_mul_u32_u24 v192, 0xc0, v192\n"
    "v_and_b32 v193, 14, v0\n"
    "v_lshl_or_b32 v192, v193, 2, v192\n"
    "v_lshl_or_b32 v192, v3, 12, v192\n"
    "v_xor_b32 v5, s46, v192\n"
    "v_xor_b32 v6, 64, v5\n"
    "s_lshl_b32 s47, s30, 12\n"
    "v_and_or_b32 v193, v0, 16, v4\n"
    "v_lshl_or_b32 v193, v193, 3, s47\n"
    "v_lshl_or_b32 v7, v3, 9, v193\n"
    "v_xor_b32 v8, 0xc0, v7\n"
    "s_or_b32 s47, s47, s45\n"
    "v_and_b32 v192, 1, v4\n"
    "v_mul_u32_u24 v192, 0x140, v192\n"
    "v_and_b32 v193, 14, v4\n"
    "v_lshl_or_b32 v192, v193, 2, v192\n"
    "v_lshl_or_b32 v192, v3, 10, v192\n"
    "v_xor_b32 v9, s47, v192\n"
    "v_xor_b32 v10, 64, v9\n"
    "v_xor_b32 v11, 0x80, v9\n"
    "v_xor_b32 v12, 0xc0, v9\n"
    "s_lshl_b32 s47, s30, 9\n"
    "v_and_b32 v192, 31, v0\n"
    "v_lshl_or_b32 v192, v192, 3, s47\n"
    "v_lshl_or_b32 v240, v3, 13, v192\n"
    "v_xor_b32 v241, 0x80, v240\n"
    "v_xor_b32 v242, 0x140, v240\n"
    "v_xor_b32 v243, 0x1c0, v240\n"
    "s_and_b32 s46, s60, 2\n"
    "s_sub_u32 s46, s29, s46\n"
    "v_lshl_or_b32 v192, v3, 5, v4\n"
    "v_lshlrev_b32 v192, 2, v192\n"
    "v_add_u32 v248, s46, v192\n"
    "v_sub_u32 v249, s46, v192\n"
    "s_and_b32 s40, s32, 0x60\n"
    "s_add_u32 s40, s40, s32\n"
    "s_lshl_b32 s40, s40, 8\n"
    "s_lshl_b32 s44, s30, 20\n"
    "s_add_u32 s40, s40, s44\n"
    "s_and_b32 s44, s40, 0x400\n"
    "v_or_b32 v13, s44, v13\n"
    "v_bfe_i32 v192, v0, 4, 1\n"
    "v_and_b32 v192, 0x400, v192\n"
    "v_xor_b32 v13, v192, v13\n"
    "s_and_b32 s44, s40, 0x600\n"
    "v_or_b32 v14, s44, v14\n"
    "v_bfe_i32 v192, v0, 4, 1\n"
    "v_and_b32 v192, 0x200, v192\n"
    "v_xor_b32 v14, v192, v14\n"
    "v_bfe_i32 v192, v0, 5, 1\n"
    "v_and_b32 v192, 0x400, v192\n"
    "v_xor_b32 v14, v192, v14\n"
    "s_andn2_b32 s54, s40, 0x600\n"
    "s_andn2_b32 s40, s40, 0x400\n"
    "s_xor_b32 s41, s40, 0x10100\n"
    "s_xor_b32 s42, s40, 0x20200\n"
    "s_xor_b32 s43, s40, 0x30300\n"
    "v_readlane_b32 s64, v153, 0\n"
    "v_readlane_b32 s65, v153, 1\n"
    "v_mov_b32 v128, 0\n"
    "v_mov_b32 v129, 0\n"
    "v_mov_b32 v130, 0\n"
    "v_mov_b32 v131, 0\n"
    "v_mov_b32 v132, 0\n"
    "v_mov_b32 v133, 0\n"
    "v_mov_b32 v134, 0\n"
    "v_mov_b32 v135, 0\n"
    "v_mov_b32 v136, 0\n"
    "v_mov_b32 v137, 0\n"
    "v_mov_b32 v138, 0\n"
    "v_mov_b32 v139, 0\n"
    "v_mov_b32 v140, 0\n"
    "v_mov_b32 v141, 0\n"
    "v_mov_b32 v142, 0\n"
    "v_mov_b32 v143, 0\n"
    "v_mov_b32 v144, 0\n"
    "v_mov_b32 v145, 0\n"
    "v_mov_b32 v146, 0\n"
    "v_mov_b32 v147, 0\n"
    "v_mov_b32 v148, 0\n"
    "v_mov_b32 v149, 0\n"
    "v_mov_b32 v150, 0\n"
    "v_mov_b32 v151, 0\n"
    "v_mov_b32 v152, 0\n"
    "v_mov_b32 v153, 0\n"
    "v_mov_b32 v154, 0\n"
    "v_mov_b32 v155, 0\n"
    "v_mov_b32 v156, 0\n"
    "v_mov_b32 v157, 0\n"
    "v_mov_b32 v158, 0\n"
    "v_mov_b32 v159, 0\n"
    "s_cmp_eq_u32 s37, 0\n"
    "s_cbranch_scc1 .Lspa_%=\n"
    ".Lspa_ret_%=:\n"
    "s_mov_b32 s68, 262144\n"
    "s_mov_b64 exec, 0xffff\n"
    "s_branch .Lp1w_%=\n"
    ".Lp1_%=:\n"
    "v_mov_b32 v236, 0\n"
    "v_mov_b32 v237, 0\n"
    "buffer_atomic_or_x2 v[236:237], v244, s[12:15], s69 offen sc0\n"
    ".Lp1w_%=:\n"
    "s_sub_u32 s68, s68, 1\n"
    "s_waitcnt vmcnt(0)\n"
    "v_and_b32 v254, s57, v236\n"
    "v_or_b32 v236, 0xff, v236\n"
    "v_cmp_eq_u32 s[50:51], 0, v254\n"
    "v_cmp_ne_u64 vcc, s[38:39], v[236:237]\n"
    "s_cbranch_scc1 .Lp1x_%=\n"
    "s_cbranch_vccnz .Lp1_%=\n"
    ".Lp1x_%=:\n"
    "s_mov_b64 exec, -1\n"
    "s_lshl_b32 s44, s50, 13\n"
    "v_lshrrev_b32 v208, v250, s44\n"
    "v_and_or_b32 v208, v208, s56, v13\n"
    "buffer_load_dwordx4 v[192:195], v208, s[8:11], s40 offen sc1\n"
    "s_lshl_b32 s44, s50, 12\n"
    "v_lshrrev_b32 v209, v250, s44\n"
    "v_and_or_b32 v209, v209, s56, v13\n"
    "buffer_load_dwordx4 v[196:199], v209, s[8:11], s41 offen sc1\n"
    "s_lshl_b32 s44, s50, 11\n"
    "v_lshrrev_b32 v210, v250, s44\n"
    "v_and_or_b32 v210, v210, s56, v13\n"
    "buffer_load_dwordx4 v[200:203], v210, s[8:11], s42 offen sc1\n"
    "s_lshl_b32 s44, s50, 10\n"
    "v_lshrrev_b32 v211, v250, s44\n"
    "v_and_or_b32 v211, v211, s56, v13\n"
    "buffer_load_dwordx4 v[204:207], v211, s[8:11], s43 offen sc1\n"
    "s_lshl_b32 s44, s50, 13\n"
    "v_lshrrev_b32 v13, v251, s44\n"
    "v_and_or_b32 v13, v13, s56, v14\n"
    "s_lshl_b32 s44, s50, 12\n"
    "v_lshrrev_b32 v246, v251, s44\n"
    "v_and_or_b32 v246, v246, s56, v14\n"
    "s_lshl_b32 s44, s50, 5\n"
    "v_lshrrev_b32 v247, v251, s44\n"
    "v_and_or_b32 v247, v247, s56, v14\n"
    "s_lshl_b32 s44, s50, 4\n"
    "v_lshrrev_b32 v252, v251, s44\n"
    "v_and_or_b32 v252, v252, s56, v14\n"
    "s_xor_b32 s40, s54, 0x0\n"
    "s_xor_b32 s41, s54, 0x10100\n"
    "s_xor_b32 s42, s54, 0x80000\n"
    "s_xor_b32 s43, s54, 0x90100\n"
    "ds_read_u16_d16_hi v128, v248 offset:0\n"
    "ds_read_u16_d16_hi v129, v248 offset:512\n"
    "ds_read_u16_d16_hi v144, v248 offset:1024\n"
    "ds_read_u16_d16_hi v145, v248 offset:1536\n"
    "ds_read_u16_d16_hi v132, v248 offset:64\n"
    "ds_read_u16_d16_hi v133, v248 offset:576\n"
    "ds_read_u16_d16_hi v148, v248 offset:1088\n"
    "ds_read_u16_d16_hi v149, v248 offset:1600\n"
    "ds_read_u16_d16_hi v136, v248 offset:2\n"
    "ds_read_u16_d16_hi v137, v248 offset:514\n"
    "ds_read_u16_d16_hi v152, v248 offset:1026\n"
    "ds_read_u16_d16_hi v153, v248 offset:1538\n"
    "ds_read_u16_d16_hi v140, v248 offset:66\n"
    "ds_read_u16_d16_hi v141, v248 offset:578\n"
    "ds_read_u16_d16_hi v156, v248 offset:1090\n"
    "ds_read_u16_d16_hi v157, v248 offset:1602\n"
    "v_lshlrev_b32 v161, 16, v161\n"
    "v_lshlrev_b32 v162, 16, v162\n"
    "v_mul_f32 v161, 0.5, v161\n"
    "v_mul_f32 v162, 0.5, v162\n"
    "s_nop 1\n"
    "v_add_f32_dpp v163, v161, v161 quad_perm:[1,0,3,2] row_mask:0xf bank_mask:0xf\n"
    "v_subrev_f32_dpp v164, v162, v162 quad_perm:[1,0,3,2] row_mask:0xf bank_mask:0xf\n"
    "v_readlane_b32 s60, v163, 0\n"
    "v_readlane_b32 s61, v164, 0\n"
    "v_readlane_b32 s62, v163, 2\n"
    "v_readlane_b32 s63, v164, 2\n"
    "s_waitcnt lgkmcnt(0)\n"
    "v_writelane_b32 v128, s60, 0\n"
    "v_writelane_b32 v144, s61, 0\n"
    "s_waitcnt vmcnt(3)\n"
    "v_mov_b32 v208, v192\n"
    "v_mov_b32 v216, v194\n"
    "v_mov_b32 v212, v193\n"
    "v_mov_b32 v220, v195\n"
    "s_waitcnt vmcnt(2)\n"
    "v_mov_b32 v209, v196\n"
    "v_mov_b32 v217, v198\n"
    "v_mov_b32 v213, v197\n"
    "v_mov_b32 v221, v199\n"
    "s_waitcnt vmcnt(1)\n"
    "v_mov_b32 v210, v200\n"
    "v_mov_b32 v218, v202\n"
    "v_mov_b32 v214, v201\n"
    "v_mov_b32 v222, v203\n"
    "s_waitcnt vmcnt(0)\n"
    "v_mov_b32 v211, v204\n"
    "v_mov_b32 v219, v206\n"
    "v_mov_b32 v215, v205\n"
    "v_mov_b32 v223, v207\n"
    "v_mfma_f32_16x16x32_bf16 v[160:163], v[112:115], v[208:211], 0\n"
    "s_add_u32 m0, s29, 2048\n"
    "v_mfma_f32_16x16x32_bf16 v[168:171], v[112:115], v[216:219], 0\n"
    "buffer_load_dwordx4 v15, s[20:23], s35 offen nt lds\n"
    "v_mfma_f32_16x16x32_bf16 v[176:179], v[116:119], v[208:211], 0\n"
    "s_add_u32 m0, s29, 3072\n"
    "v_mfma_f32_16x16x32_bf16 v[184:187], v[116:119], v[216:219], 0\n"
    "buffer_load_dwordx4 v15, s[24:27], s35 offen nt lds\n"
    "v_mfma_f32_16x16x32_bf16 v[164:167], v[112:115], v[212:215], 0\n"
    "v_mfma_f32_16x16x32_bf16 v[172:175], v[112:115], v[220:223], 0\n"
    "v_cvt_pk_bf16_f32 v224, v160, v161\n"
    "v_cvt_pk_bf16_f32 v225, v168, v169\n"
    "ds_write_b64 v5, v[224:225] offset:0\n"
    "v_mfma_f32_16x16x32_bf16 v[180:183], v[116:119], v[212:215], 0\n"
    "v_cvt_pk_bf16_f32 v226, v162, v163\n"
    "v_cvt_pk_bf16_f32 v227, v170, v171\n"
    "ds_write_b64 v5, v[226:227] offset:2048\n"
    "v_mfma_f32_16x16x32_bf16 v[188:191], v[116:119], v[220:223], 0\n"
    "v_cvt_pk_bf16_f32 v228, v176, v177\n"
    "v_cvt_pk_bf16_f32 v229, v184, v185\n"
    "ds_write_b64 v5, v[228:229] offset:16384\n"
    "v_cvt_pk_bf16_f32 v230, v178, v179\n"
    "v_cvt_pk_bf16_f32 v231, v186, v187\n"
    "ds_write_b64 v5, v[230:231] offset:18432\n"
    "v_cvt_pk_bf16_f32 v232, v164, v165\n"
    "v_cvt_pk_bf16_f32 v233, v172, v173\n"
    "ds_write_b64 v6, v[232:233] offset:0\n"
    "v_cvt_pk_bf16_f32 v234, v166, v167\n"
    "v_cvt_pk_bf16_f32 v235, v174, v175\n"
    "ds_write_b64 v6, v[234:235] offset:2048\n"
    "v_cvt_pk_bf16_f32 v236, v180, v181\n"
    "v_cvt_pk_bf16_f32 v237, v188, v189\n"
    "ds_write_b64 v6, v[236:237] offset:16384\n"
    "v_cvt_pk_bf16_f32 v238, v182, v183\n"
    "v_cvt_pk_bf16_f32 v239, v190, v191\n"
    "ds_write_b64 v6, v[238:239] offset:18432\n"
    "s_waitcnt lgkmcnt(0)\n"
    "s_barrier\n"
    "ds_read_b64 v[224:225], v7 offset:0\n"
    "ds_read_b64 v[226:227], v7 offset:256\n"
    "ds_read_b64 v[228:229], v8 offset:0\n"
    "ds_read_b64 v[230:231], v8 offset:256\n"
    "ds_read_b64 v[232:233], v7 offset:2048\n"
    "ds_read_b64 v[234:235], v7 offset:2304\n"
    "ds_read_b64 v[236:237], v8 offset:2048\n"
    "ds_read_b64 v[238:239], v8 offset:2304\n"
    "s_setprio 1\n"
    "s_waitcnt lgkmcnt(6)\n"
    "v_mfma_f32_16x16x32_bf16 v[160:163], v[224:227], v[16:19], 0\n"
    "v_mfma_f32_16x16x32_bf16 v[164:167], v[224:227], v[20:23], 0\n"
    "s_waitcnt lgkmcnt(4)\n"
    "v_mfma_f32_16x16x32_bf16 v[168:171], v[228:231], v[16:19], 0\n"
    "v_mfma_f32_16x16x32_bf16 v[172:175], v[228:231], v[20:23], 0\n"
    "s_waitcnt lgkmcnt(2)\n"
    "v_mfma_f32_16x16x32_bf16 v[176:179], v[232:235], v[24:27], 0\n"
    "v_mfma_f32_16x16x32_bf16 v[180:183], v[232:235], v[28:31], 0\n"
    "s_waitcnt lgkmcnt(0)\n"
    "v_mfma_f32_16x16x32_bf16 v[184:187], v[236:239], v[24:27], 0\n"
    "v_mfma_f32_16x16x32_bf16 v[188:191], v[236:239], v[28:31], 0\n"
    "s_setprio 0\n"
    "v_fmac_f32 v160, v80, v164\n"
    "v_fmac_f32 v161, v81, v165\n"
    "v_fmac_f32 v162, v82, v166\n"
    "v_fmac_f32 v163, v83, v167\n"
    "v_fmac_f32 v164, v96, v160\n"
    "v_fmac_f32 v165, v97, v161\n"
    "v_fmac_f32 v166, v98, v162\n"
    "v_fmac_f32 v167, v99, v163\n"
    "v_fmac_f32 v160, v80, v164\n"
    "v_fmac_f32 v161, v81, v165\n"
    "v_fmac_f32 v162, v82, v166\n"
    "v_fmac_f32 v163, v83, v167\n"
    "v_fmac_f32 v168, v84, v172\n"
    "v_fmac_f32 v169, v85, v173\n"
    "v_fmac_f32 v170, v86, v174\n"
    "v_fmac_f32 v171, v87, v175\n"
    "v_fmac_f32 v172, v100, v168\n"
    "v_fmac_f32 v173, v101, v169\n"
    "v_fmac_f32 v174, v102, v170\n"
    "v_fmac_f32 v175, v103, v171\n"
    "v_fmac_f32 v168, v84, v172\n"
    "v_fmac_f32 v169, v85, v173\n"
    "v_fmac_f32 v170, v86, v174\n"
    "v_fmac_f32 v171, v87, v175\n"
    "v_fmac_f32 v176, v88, v180\n"
    "v_fmac_f32 v177, v89, v181\n"
    "v_fmac_f32 v178, v90, v182\n"
    "v_fmac_f32 v179, v91, v183\n"
    "v_fmac_f32 v180, v104, v176\n"
    "v_fmac_f32 v181, v105, v177\n"
    "v_fmac_f32 v182, v106, v178\n"
    "v_fmac_f32 v183, v107, v179\n"
    "v_fmac_f32 v176, v88, v180\n"
    "v_fmac_f32 v177, v89, v181\n"
    "v_fmac_f32 v178, v90, v182\n"
    "v_fmac_f32 v179, v91, v183\n"
    "v_fmac_f32 v184, v92, v188\n"
    "v_fmac_f32 v185, v93, v189\n"
    "v_fmac_f32 v186, v94, v190\n"
    "v_fmac_f32 v187, v95, v191\n"
    "v_fmac_f32 v188, v108, v184\n"
    "v_fmac_f32 v189, v109, v185\n"
    "v_fmac_f32 v190, v110, v186\n"
    "v_fmac_f32 v191, v111, v187\n"
    "v_fmac_f32 v184, v92, v188\n"
    "v_fmac_f32 v185, v93, v189\n"
    "v_fmac_f32 v186, v94, v190\n"
    "v_fmac_f32 v187, v95, v191\n"
    "s_setprio 1\n"
    "v_cvt_pk_bf16_f32 v224, v160, v164\n"
    "v_cvt_pk_bf16_f32 v225, v161, v165\n"
    "v_cvt_pk_bf16_f32 v226, v162, v166\n"
    "v_cvt_pk_bf16_f32 v227, v163, v167\n"
    "v_cvt_pk_bf16_f32 v228, v168, v172\n"
    "v_cvt_pk_bf16_f32 v229, v169, v173\n"
    "v_mfma_f32_16x16x32_bf16 v[192:195], v[48:51], v[224:227], 0\n"
    "v_cvt_pk_bf16_f32 v230, v170, v174\n"
    "v_mfma_f32_16x16x32_bf16 v[196:199], v[52:55], v[224:227], 0\n"
    "v_cvt_pk_bf16_f32 v231, v171, v175\n"
    "v_mfma_f32_16x16x32_bf16 v[200:203], v[64:67], v[224:227], 0\n"
    "v_cvt_pk_bf16_f32 v232, v176, v180\n"
    "v_mfma_f32_16x16x32_bf16 v[204:207], v[68:71], v[224:227], 0\n"
    "v_cvt_pk_bf16_f32 v233, v177, v181\n"
    "v_mfma_f32_16x16x32_bf16 v[192:195], v[56:59], v[228:231], v[192:195]\n"
    "v_cvt_pk_bf16_f32 v234, v178, v182\n"
    "v_mfma_f32_16x16x32_bf16 v[196:199], v[60:63], v[228:231], v[196:199]\n"
    "v_cvt_pk_bf16_f32 v235, v179, v183\n"
    "v_mfma_f32_16x16x32_bf16 v[200:203], v[72:75], v[228:231], v[200:203]\n"
    "v_cvt_pk_bf16_f32 v236, v184, v188\n"
    "v_mfma_f32_16x16x32_bf16 v[204:207], v[76:79], v[228:231], v[204:207]\n"
    "v_cvt_pk_bf16_f32 v237, v185, v189\n"
    "v_mfma_f32_16x16x32_bf16 v[208:211], v[48:51], v[232:235], 0\n"
    "v_cvt_pk_bf16_f32 v238, v186, v190\n"
    "s_waitcnt vmcnt(0)\n"
    "v_mfma_f32_16x16x32_bf16 v[212:215], v[52:55], v[232:235], 0\n"
    "v_cvt_pk_bf16_f32 v239, v187, v191\n"
    "ds_read_u16_d16_hi v130, v249 offset:3072\n"
    "v_mfma_f32_16x16x32_bf16 v[216:219], v[64:67], v[232:235], 0\n"
    "ds_read_u16_d16_hi v131, v249 offset:2560\n"
    "ds_read_u16_d16_hi v146, v249 offset:4096\n"
    "v_mfma_f32_16x16x32_bf16 v[220:223], v[68:71], v[232:235], 0\n"
    "ds_read_u16_d16_hi v147, v249 offset:3584\n"
    "ds_read_u16_d16_hi v134, v249 offset:3008\n"
    "v_mfma_f32_16x16x32_bf16 v[208:211], v[56:59], v[236:239], v[208:211]\n"
    "ds_read_u16_d16_hi v135, v249 offset:2496\n"
    "ds_read_u16_d16_hi v150, v249 offset:4032\n"
    "v_mfma_f32_16x16x32_bf16 v[212:215], v[60:63], v[236:239], v[212:215]\n"
    "ds_read_u16_d16_hi v151, v249 offset:3520\n"
    "ds_read_u16_d16_hi v138, v249 offset:3070\n"
    "v_mfma_f32_16x16x32_bf16 v[216:219], v[72:75], v[236:239], v[216:219]\n"
    "ds_read_u16_d16_hi v139, v249 offset:2558\n"
    "ds_read_u16_d16_hi v154, v249 offset:4094\n"
    "v_mfma_f32_16x16x32_bf16 v[220:223], v[76:79], v[236:239], v[220:223]\n"
    "ds_read_u16_d16_hi v155, v249 offset:3582\n"
    "ds_read_u16_d16_hi v142, v249 offset:3006\n"
    "s_setprio 0\n"
    "ds_read_u16_d16_hi v143, v249 offset:2494\n"
    "ds_read_u16_d16_hi v158, v249 offset:4030\n"
    "ds_read_u16_d16_hi v159, v249 offset:3518\n"
    "v_mul_f32 v160, v196, v128\n"
    "v_mul_f32 v161, v197, v129\n"
    "v_mul_f32 v164, v192, v128\n"
    "v_mul_f32 v165, v193, v129\n"
    "v_fmac_f32 v160, v192, v144\n"
    "v_fmac_f32 v161, v193, v145\n"
    "v_fma_f32 v164, -v196, v144, v164\n"
    "v_fma_f32 v165, -v197, v145, v165\n"
    "v_mul_f32 v168, v204, v132\n"
    "v_mul_f32 v169, v205, v133\n"
    "v_mul_f32 v172, v200, v132\n"
    "v_mul_f32 v173, v201, v133\n"
    "v_fmac_f32 v168, v200, v148\n"
    "v_fmac_f32 v169, v201, v149\n"
    "v_fma_f32 v172, -v204, v148, v172\n"
    "v_fma_f32 v173, -v205, v149, v173\n"
    "v_mul_f32 v176, v212, v136\n"
    "v_mul_f32 v177, v213, v137\n"
    "v_mul_f32 v180, v208, v136\n"
    "v_mul_f32 v181, v209, v137\n"
    "v_fmac_f32 v176, v208, v152\n"
    "v_fmac_f32 v177, v209, v153\n"
    "v_fma_f32 v180, -v212, v152, v180\n"
    "v_fma_f32 v181, -v213, v153, v181\n"
    "v_mul_f32 v184, v220, v140\n"
    "v_mul_f32 v185, v221, v141\n"
    "v_mul_f32 v188, v216, v140\n"
    "v_mul_f32 v189, v217, v141\n"
    "v_fmac_f32 v184, v216, v156\n"
    "v_fmac_f32 v185, v217, v157\n"
    "v_fma_f32 v188, -v220, v156, v188\n"
    "v_fma_f32 v189, -v221, v157, v189\n"
    "s_waitcnt lgkmcnt(0)\n"
    "v_writelane_b32 v130, s62, 0\n"
    "v_writelane_b32 v146, s63, 0\n"
    "v_mul_f32 v162, v198, v130\n"
    "v_mul_f32 v163, v199, v131\n"
    "v_mul_f32 v166, v194, v130\n"
    "v_mul_f32 v167, v195, v131\n"
    "v_fma_f32 v162, -v194, v146, v162\n"
    "v_fma_f32 v163, -v195, v147, v163\n"
    "v_fmac_f32 v166, v198, v146\n"
    "v_fmac_f32 v167, v199, v147\n"
    "v_mul_f32 v170, v206, v134\n"
    "v_mul_f32 v171, v207, v135\n"
    "v_mul_f32 v174, v202, v134\n"
    "v_mul_f32 v175, v203, v135\n"
    "v_fma_f32 v170, -v202, v150, v170\n"
    "v_fma_f32 v171, -v203, v151, v171\n"
    "v_fmac_f32 v174, v206, v150\n"
    "v_fmac_f32 v175, v207, v151\n"
    "v_mul_f32 v178, v214, v138\n"
    "v_mul_f32 v179, v215, v139\n"
    "v_mul_f32 v182, v210, v138\n"
    "v_mul_f32 v183, v211, v139\n"
    "v_fma_f32 v178, -v210, v154, v178\n"
    "v_fma_f32 v179, -v211, v155, v179\n"
    "v_fmac_f32 v182, v214, v154\n"
    "v_fmac_f32 v183, v215, v155\n"
    "v_mul_f32 v186, v222, v142\n"
    "v_mul_f32 v187, v223, v143\n"
    "v_mul_f32 v190, v218, v142\n"
    "v_mul_f32 v191, v219, v143\n"
    "v_fma_f32 v186, -v218, v158, v186\n"
    "v_fma_f32 v187, -v219, v159, v187\n"
    "v_fmac_f32 v190, v222, v158\n"
    "v_fmac_f32 v191, v223, v159\n"
    "s_cmp_eq_u32 s37, 0\n"
    "s_cbranch_scc1 .Lspb_%=\n"
    ".Lspb_ret_%=:\n"
    "s_setprio 1\n"
    "v_cvt_pk_bf16_f32 v224, v160, v164\n"
    "v_cvt_pk_bf16_f32 v225, v161, v165\n"
    "v_cvt_pk_bf16_f32 v226, v162, v166\n"
    "v_cvt_pk_bf16_f32 v227, v163, v167\n"
    "v_cvt_pk_bf16_f32 v228, v168, v172\n"
    "v_cvt_pk_bf16_f32 v229, v169, v173\n"
    "v_mfma_f32_16x16x32_bf16 v[192:195], v[224:227], v[48:51], 0\n"
    "v_cvt_pk_bf16_f32 v230, v170, v174\n"
    "v_mfma_f32_16x16x32_bf16 v[196:199], v[224:227], v[52:55], 0\n"
    "v_cvt_pk_bf16_f32 v231, v171, v175\n"
    "v_mfma_f32_16x16x32_bf16 v[200:203], v[224:227], v[64:67], 0\n"
    "v_cvt_pk_bf16_f32 v232, v176, v180\n"
    "v_mfma_f32_16x16x32_bf16 v[204:207], v[224:227], v[68:71], 0\n"
    "v_cvt_pk_bf16_f32 v233, v177, v181\n"
    "v_mfma_f32_16x16x32_bf16 v[192:195], v[228:231], v[56:59], v[192:195]\n"
    "v_cvt_pk_bf16_f32 v234, v178, v182\n"
    "v_mfma_f32_16x16x32_bf16 v[196:199], v[228:231], v[60:63], v[196:199]\n"
    "v_cvt_pk_bf16_f32 v235, v179, v183\n"
    "v_mfma_f32_16x16x32_bf16 v[200:203], v[228:231], v[72:75], v[200:203]\n"
    "v_cvt_pk_bf16_f32 v236, v184, v188\n"
    "v_mfma_f32_16x16x32_bf16 v[204:207], v[228:231], v[76:79], v[204:207]\n"
    "v_cvt_pk_bf16_f32 v237, v185, v189\n"
    "v_mfma_f32_16x16x32_bf16 v[208:211], v[232:235], v[48:51], 0\n"
    "v_cvt_pk_bf16_f32 v238, v186, v190\n"
    "v_mfma_f32_16x16x32_bf16 v[212:215], v[232:235], v[52:55], 0\n"
    "v_cvt_pk_bf16_f32 v239, v187, v191\n"
    "v_mfma_f32_16x16x32_bf16 v[216:219], v[232:235], v[64:67], 0\n"
    "v_mfma_f32_16x16x32_bf16 v[220:223], v[232:235], v[68:71], 0\n"
    "v_mfma_f32_16x16x32_bf16 v[208:211], v[236:239], v[56:59], v[208:211]\n"
    "v_mfma_f32_16x16x32_bf16 v[212:215], v[236:239], v[60:63], v[212:215]\n"
    "v_mfma_f32_16x16x32_bf16 v[216:219], v[236:239], v[72:75], v[216:219]\n"
    "v_mfma_f32_16x16x32_bf16 v[220:223], v[236:239], v[76:79], v[220:223]\n"
    "s_setprio 0\n"
    "v_fmac_f32 v192, v80, v196\n"
    "v_fmac_f32 v193, v81, v197\n"
    "v_fmac_f32 v194, v82, v198\n"
    "v_fmac_f32 v195, v83, v199\n"
    "v_fmac_f32 v196, v96, v192\n"
    "v_fmac_f32 v197, v97, v193\n"
    "v_fmac_f32 v198, v98, v194\n"
    "v_fmac_f32 v199, v99, v195\n"
    "v_fmac_f32 v192, v80, v196\n"
    "v_fmac_f32 v193, v81, v197\n"
    "v_fmac_f32 v194, v82, v198\n"
    "v_fmac_f32 v195, v83, v199\n"
    "v_fmac_f32 v200, v88, v204\n"
    "v_fmac_f32 v201, v89, v205\n"
    "v_fmac_f32 v202, v90, v206\n"
    "v_fmac_f32 v203, v91, v207\n"
    "v_fmac_f32 v204, v104, v200\n"
    "v_fmac_f32 v205, v105, v201\n"
    "v_fmac_f32 v206, v106, v202\n"
    "v_fmac_f32 v207, v107, v203\n"
    "v_fmac_f32 v200, v88, v204\n"
    "v_fmac_f32 v201, v89, v205\n"
    "v_fmac_f32 v202, v90, v206\n"
    "v_fmac_f32 v203, v91, v207\n"
    "v_fmac_f32 v208, v84, v212\n"
    "v_fmac_f32 v209, v85, v213\n"
    "v_fmac_f32 v210, v86, v214\n"
    "v_fmac_f32 v211, v87, v215\n"
    "v_fmac_f32 v212, v100, v208\n"
    "v_fmac_f32 v213, v101, v209\n"
    "v_fmac_f32 v214, v102, v210\n"
    "v_fmac_f32 v215, v103, v211\n"
    "v_fmac_f32 v208, v84, v212\n"
    "v_fmac_f32 v209, v85, v213\n"
    "v_fmac_f32 v210, v86, v214\n"
    "v_fmac_f32 v211, v87, v215\n"
    "v_fmac_f32 v216, v92, v220\n"
    "v_fmac_f32 v217, v93, v221\n"
    "v_fmac_f32 v218, v94, v222\n"
    "v_fmac_f32 v219, v95, v223\n"
    "v_fmac_f32 v220, v108, v216\n"
    "v_fmac_f32 v221, v109, v217\n"
    "v_fmac_f32 v222, v110, v218\n"
    "v_fmac_f32 v223, v111, v219\n"
    "v_fmac_f32 v216, v92, v220\n"
    "v_fmac_f32 v217, v93, v221\n"
    "v_fmac_f32 v218, v94, v222\n"
    "v_fmac_f32 v219, v95, v223\n"
    "s_setprio 1\n"
    "v_cvt_pk_bf16_f32 v224, v196, v192\n"
    "v_cvt_pk_bf16_f32 v225, v197, v193\n"
    "v_cvt_pk_bf16_f32 v226, v198, v194\n"
    "v_cvt_pk_bf16_f32 v227, v199, v195\n"
    "v_cvt_pk_bf16_f32 v232, v212, v208\n"
    "v_cvt_pk_bf16_f32 v233, v213, v209\n"
    "v_mfma_f32_16x16x32_bf16 v[160:163], v[32:35], v[224:227], 0\n"
    "v_cvt_pk_bf16_f32 v234, v214, v210\n"
    "v_cvt_pk_bf16_f32 v235, v215, v211\n"
    "v_mfma_f32_16x16x32_bf16 v[164:167], v[36:39], v[224:227], 0\n"
    "v_cvt_pk_bf16_f32 v228, v204, v200\n"
    "v_cvt_pk_bf16_f32 v229, v205, v201\n"
    "v_mfma_f32_16x16x32_bf16 v[176:179], v[40:43], v[232:235], 0\n"
    "v_cvt_pk_bf16_f32 v230, v206, v202\n"
    "v_cvt_pk_bf16_f32 v231, v207, v203\n"
    "v_mfma_f32_16x16x32_bf16 v[180:183], v[44:47], v[232:235], 0\n"
    "v_cvt_pk_bf16_f32 v236, v220, v216\n"
    "v_cvt_pk_bf16_f32 v237, v221, v217\n"
    "v_mfma_f32_16x16x32_bf16 v[168:171], v[32:35], v[228:231], 0\n"
    "v_cvt_pk_bf16_f32 v238, v222, v218\n"
    "v_cvt_pk_bf16_f32 v239, v223, v219\n"
    "v_mfma_f32_16x16x32_bf16 v[172:175], v[36:39], v[228:231], 0\n"
    "v_cvt_pk_bf16_f32 v16, v160, v164\n"
    "s_nop 0\n"
    "v_cvt_pk_bf16_f32 v17, v176, v180\n"
    "v_mfma_f32_16x16x32_bf16 v[184:187], v[40:43], v[236:239], 0\n"
    "ds_write_b64 v9, v[16:17] offset:0\n"
    "v_cvt_pk_bf16_f32 v18, v161, v165\n"
    "v_mfma_f32_16x16x32_bf16 v[188:191], v[44:47], v[236:239], 0\n"
    "v_cvt_pk_bf16_f32 v19, v177, v181\n"
    "ds_write_b64 v10, v[18:19] offset:0\n"
    "v_cvt_pk_bf16_f32 v20, v162, v166\n"
    "v_cvt_pk_bf16_f32 v21, v178, v182\n"
    "ds_write_b64 v9, v[20:21] offset:512\n"
    "v_cvt_pk_bf16_f32 v22, v163, v167\n"
    "v_cvt_pk_bf16_f32 v23, v179, v183\n"
    "ds_write_b64 v10, v[22:23] offset:512\n"
    "v_cvt_pk_bf16_f32 v24, v168, v172\n"
    "v_cvt_pk_bf16_f32 v25, v184, v188\n"
    "ds_write_b64 v11, v[24:25] offset:0\n"
    "v_cvt_pk_bf16_f32 v26, v169, v173\n"
    "v_cvt_pk_bf16_f32 v27, v185, v189\n"
    "ds_write_b64 v12, v[26:27] offset:0\n"
    "v_cvt_pk_bf16_f32 v28, v170, v174\n"
    "v_cvt_pk_bf16_f32 v29, v186, v190\n"
    "ds_write_b64 v11, v[28:29] offset:512\n"
    "v_cvt_pk_bf16_f32 v30, v171, v175\n"
    "v_cvt_pk_bf16_f32 v31, v187, v191\n"
    "ds_write_b64 v12, v[30:31] offset:512\n"
    "s_setprio 0\n"
    "s_waitcnt lgkmcnt(0)\n"
    "s_barrier\n"
    "ds_read_b64 v[192:193], v240\n"
    "ds_read_b64 v[194:195], v240 offset:4096\n"
    "ds_read_b64 v[196:197], v241\n"
    "ds_read_b64 v[198:199], v241 offset:4096\n"
    "ds_read_b64 v[200:201], v242\n"
    "ds_read_b64 v[202:203], v242 offset:4096\n"
    "ds_read_b64 v[204:205], v243\n"
    "ds_read_b64 v[206:207], v243 offset:4096\n"
    "s_waitcnt lgkmcnt(6)\n"
    "v_mfma_f32_16x16x32_bf16 v[160:163], v[120:123], v[192:195], 0\n"
    "s_waitcnt lgkmcnt(4)\n"
    "v_mfma_f32_16x16x32_bf16 v[164:167], v[120:123], v[196:199], 0\n"
    "s_waitcnt lgkmcnt(2)\n"
    "v_mfma_f32_16x16x32_bf16 v[168:171], v[120:123], v[200:203], 0\n"
    "s_waitcnt lgkmcnt(0)\n"
    "v_mfma_f32_16x16x32_bf16 v[172:175], v[120:123], v[204:207], 0\n"
    "v_mfma_f32_16x16x32_bf16 v[176:179], v[124:127], v[192:195], 0\n"
    "s_nop 0\n"
    "v_cvt_pk_bf16_f32 v224, v160, v161\n"
    "s_nop 0\n"
    "v_cvt_pk_bf16_f32 v225, v164, v165\n"
    "v_mfma_f32_16x16x32_bf16 v[180:183], v[124:127], v[196:199], 0\n"
    "v_cvt_pk_bf16_f32 v226, v168, v169\n"
    "v_cvt_pk_bf16_f32 v228, v162, v163\n"
    "v_cvt_pk_bf16_f32 v229, v166, v167\n"
    "v_cvt_pk_bf16_f32 v227, v172, v173\n"
    "buffer_store_dwordx4 v[224:227], v13, s[8:11], s40 offen sc1\n"
    "v_mfma_f32_16x16x32_bf16 v[184:187], v[124:127], v[200:203], 0\n"
    "v_cvt_pk_bf16_f32 v230, v170, v171\n"
    "v_cvt_pk_bf16_f32 v231, v174, v175\n"
    "v_mfma_f32_16x16x32_bf16 v[188:191], v[124:127], v[204:207], 0\n"
    "buffer_store_dwordx4 v[228:231], v246, s[8:11], s41 offen sc1\n"
    "v_cvt_pk_bf16_f32 v232, v176, v177\n"
    "v_cvt_pk_bf16_f32 v233, v180, v181\n"
    "v_cvt_pk_bf16_f32 v236, v178, v179\n"
    "v_cvt_pk_bf16_f32 v237, v182, v183\n"
    "v_cvt_pk_bf16_f32 v234, v184, v185\n"
    "v_cvt_pk_bf16_f32 v238, v186, v187\n"
    "s_nop 0\n"
    "v_cvt_pk_bf16_f32 v235, v188, v189\n"
    "buffer_store_dwordx4 v[232:235], v247, s[8:11], s42 offen sc1\n"
    "v_cvt_pk_bf16_f32 v239, v190, v191\n"
    "buffer_store_dwordx4 v[236:239], v252, s[8:11], s43 offen sc1\n"
    "v_lshrrev_b32 v32, 1, v2\n"
    "v_and_b32 v33, 1, v0\n"
    "v_lshrrev_b32 v42, 1, v3\n"
    "v_cmp_ne_u32 vcc, v42, v33\n"
    "v_lshlrev_b32 v51, 31, v32\n"
    "v_lshlrev_b32 v46, 31, v0\n"
    "v_cvt_f32_u32 v32, v32\n"
    "v_cvt_f32_u32 v33, v33\n"
    "v_cvt_f32_u32 v34, v250\n"
    "v_cvt_f32_u32 v52, s30\n"
    "v_cvt_f32_u32 v35, v2\n"
    "s_cmp_eq_u32 s30, 0\n"
    "s_cselect_b64 vcc, vcc, 0\n"
    "s_cselect_b64 s[50:51], 0x5555, 0\n"
    "s_cselect_b32 s44, 0x3ef00000, 0\n"
    "s_cselect_b32 s45, 0, 0x3e800000\n"
    "s_mov_b64 s[52:53], 0xffff\n"
    "v_xor_b32 v51, 0x3f803f80, v51\n"
    "v_cndmask_b32 v50, -1, 0, vcc\n"
    "v_mul_f32 v52, 0xbb800000, v52\n"
    "v_fmac_f32 v52, s44, v33\n"
    "v_mul_f32 v33, s45, v33\n"
    "v_mul_f32 v35, 0xbd800000, v35\n"
    "v_xor_b32 v48, 0x35803580, v46\n"
    "v_mul_f32 v37, v35, v34\n"
    "v_mul_f32 v37, 0.5, v37\n"
    "v_add_f32 v38, v37, v35\n"
    "v_cos_f32 v42, v37\n"
    "v_sin_f32 v46, v37\n"
    "v_cos_f32 v43, v38\n"
    "v_sin_f32 v47, v38\n"
    "v_mul_f32 v42, 0x35800000, v42\n"
    "v_mul_f32 v46, 0x35800000, v46\n"
    "v_mul_f32 v43, 0x35800000, v43\n"
    "v_mul_f32 v47, 0x35800000, v47\n"
    "v_cvt_pk_bf16_f32 v56, v42, v46\n"
    "v_cvt_pk_bf16_f32 v57, v43, v47\n"
    "v_cndmask_b32 v56, v56, v48, s[52:53]\n"
    "v_mul_f32 v35, 0xbd800000, v32\n"
    "v_fma_f32 v37, v32, v52, v33\n"
    "v_fmac_f32 v37, v34, v35\n"
    "v_add_f32 v38, v37, v35\n"
    "v_add_f32 v39, v38, v35\n"
    "v_add_f32 v40, v39, v35\n"
    "v_cos_f32 v42, v37\n"
    "v_sin_f32 v46, v37\n"
    "v_cos_f32 v43, v38\n"
    "v_sin_f32 v47, v38\n"
    "v_cos_f32 v44, v39\n"
    "v_sin_f32 v48, v39\n"
    "v_cos_f32 v45, v40\n"
    "v_sin_f32 v49, v40\n"
    "v_cvt_pk_bf16_f32 v60, v42, v46\n"
    "v_cvt_pk_bf16_f32 v61, v43, v47\n"
    "v_cvt_pk_bf16_f32 v62, v44, v48\n"
    "v_cvt_pk_bf16_f32 v63, v45, v49\n"
    "v_and_b32 v60, v50, v60\n"
    "v_and_b32 v61, v50, v61\n"
    "v_and_b32 v62, v50, v62\n"
    "v_and_b32 v63, v50, v63\n"
    "v_cndmask_b32 v60, v60, v51, s[50:51]\n"
    "v_add_f32 v32, 0x41000000, v32\n"
    "v_mul_f32 v35, 0xbd800000, v32\n"
    "v_fma_f32 v37, v32, v52, v33\n"
    "v_fmac_f32 v37, v34, v35\n"
    "v_add_f32 v38, v37, v35\n"
    "v_add_f32 v39, v38, v35\n"
    "v_add_f32 v40, v39, v35\n"
    "v_cos_f32 v42, v37\n"
    "v_sin_f32 v46, v37\n"
    "v_cos_f32 v43, v38\n"
    "v_sin_f32 v47, v38\n"
    "v_cos_f32 v44, v39\n"
    "v_sin_f32 v48, v39\n"
    "v_cos_f32 v45, v40\n"
    "v_sin_f32 v49, v40\n"
    "v_cvt_pk_bf16_f32 v64, v42, v46\n"
    "v_cvt_pk_bf16_f32 v65, v43, v47\n"
    "v_cvt_pk_bf16_f32 v66, v44, v48\n"
    "v_cvt_pk_bf16_f32 v67, v45, v49\n"
    "v_and_b32 v64, v50, v64\n"
    "v_and_b32 v65, v50, v65\n"
    "v_and_b32 v66, v50, v66\n"
    "v_and_b32 v67, v50, v67\n"
    "v_cndmask_b32 v64, v64, v51, s[50:51]\n"
    "v_lshlrev_b32 v42, 4, v2\n"
    "v_lshl_or_b32 v43, v3, 10, v42\n"
    "v_and_b32 v44, 4, v250\n"
    "v_mov_b32 v252, s38\n"
    "s_lshl_b32 s44, s3, 13\n"
    "v_lshrrev_b32 v72, v44, s44\n"
    "v_and_or_b32 v72, v72, s56, v43\n"
    "s_xor_b32 s44, s66, 0\n"
    "v_xor_b32 v72, s44, v72\n"
    "s_lshl_b32 s44, s3, 12\n"
    "v_lshrrev_b32 v73, v44, s44\n"
    "v_and_or_b32 v73, v73, s56, v43\n"
    "s_xor_b32 s44, s66, 256\n"
    "v_xor_b32 v73, s44, v73\n"
    "s_lshl_b32 s44, s3, 11\n"
    "v_lshrrev_b32 v74, v44, s44\n"
    "v_and_or_b32 v74, v74, s56, v43\n"
    "s_xor_b32 s44, s66, 512\n"
    "v_xor_b32 v74, s44, v74\n"
    "s_lshl_b32 s44, s3, 10\n"
    "v_lshrrev_b32 v75, v44, s44\n"
    "v_and_or_b32 v75, v75, s56, v43\n"
    "s_xor_b32 s44, s66, 768\n"
    "v_xor_b32 v75, s44, v75\n"
    "s_lshl_b32 s44, s30, 12\n"
    "s_lshl4_add_u32 s44, s31, s44\n"
    "v_lshl_or_b32 v68, v3, 9, v42\n"
    "v_xor_b32 v68, s44, v68\n"
    "s_lshl_b32 s44, s30, 9\n"
    "v_and_or_b32 v46, v0, 16, s44\n"
    "v_lshl_or_b32 v46, v2, 5, v46\n"
    "v_lshl_or_b32 v69, v3, 13, v46\n"
    "v_xor_b32 v70, 16, v69\n"
    "v_and_b32 v71, 0x70, v42\n"
    "v_lshl_or_b32 v71, v3, 21, v71\n"
    "v_and_b32 v46, 8, v0\n"
    "v_lshl_or_b32 v71, v46, 12, v71\n"
    "s_and_b32 s60, s32, 0x70\n"
    "s_add_u32 s60, s60, s32\n"
    "s_add_u32 s60, s60, s44\n"
    "s_lshl_b32 s60, s60, 7\n"
    "s_add_u32 s61, s60, 0x80000\n"
    "s_add_u32 s62, s60, 0x100000\n"
    "s_add_u32 s63, s60, 0x180000\n"
    "s_add_u32 s69, s69, 32768\n"
    "s_lshl3_add_u32 s44, s32, s67\n"
    "s_add_u32 s44, s44, 32768\n"
    "s_cmp_lg_u32 s37, 0\n"
    "s_mov_b64 exec, 15\n"
    "s_waitcnt vmcnt(0) lgkmcnt(0)\n"
    "s_barrier\n"
    "buffer_store_dwordx2 v[252:253], v245, s[12:15], s44 offen sc1\n"
    "s_cbranch_scc1 .Lnbump_%=\n"
    "s_mov_b64 exec, 1\n"
    "buffer_store_dwordx2 v[252:253], off, s[12:15], 0 sc1\n"
    ".Lnbump_%=:\n"
    "s_mov_b32 s68, 262144\n"
    "s_mov_b64 exec, 0xffff\n"
    ".Lp2_%=:\n"
    "v_mov_b32 v236, 0\n"
    "v_mov_b32 v237, 0\n"
    "buffer_atomic_or_x2 v[236:237], v244, s[12:15], s69 offen sc0\n"
    "s_sub_u32 s68, s68, 1\n"
    "s_waitcnt vmcnt(0)\n"
    "v_cmp_ne_u64 vcc, s[38:39], v[236:237]\n"
    "s_cbranch_scc1 .Lp2x_%=\n"
    "s_cbranch_vccnz .Lp2_%=\n"
    ".Lp2x_%=:\n"
    "s_mov_b64 exec, -1\n"
    "buffer_load_dwordx4 v[192:195], v72, s[8:11], s36 offen sc1\n"
    "buffer_load_dwordx4 v[196:199], v73, s[8:11], s36 offen sc1\n"
    "buffer_load_dwordx4 v[200:203], v74, s[8:11], s36 offen sc1\n"
    "buffer_load_dwordx4 v[204:207], v75, s[8:11], s36 offen sc1\n"
    "s_waitcnt vmcnt(3)\n"
    "v_mov_b32 v208, v192\n"
    "v_mov_b32 v212, v193\n"
    "v_mov_b32 v216, v194\n"
    "v_mov_b32 v220, v195\n"
    "s_waitcnt vmcnt(2)\n"
    "v_mov_b32 v209, v196\n"
    "v_mov_b32 v213, v197\n"
    "v_mov_b32 v217, v198\n"
    "v_mov_b32 v221, v199\n"
    "s_waitcnt vmcnt(1)\n"
    "v_mov_b32 v210, v200\n"
    "v_mov_b32 v214, v201\n"
    "v_mov_b32 v218, v202\n"
    "v_mov_b32 v222, v203\n"
    "s_waitcnt vmcnt(0)\n"
    "v_mov_b32 v211, v204\n"
    "v_mov_b32 v215, v205\n"
    "v_mov_b32 v219, v206\n"
    "v_mov_b32 v223, v207\n"
    "v_mfma_f32_16x16x32_bf16 v[160:163], v[60:63], v[208:211], 0\n"
    "v_mfma_f32_16x16x32_bf16 v[164:167], v[60:63], v[212:215], 0\n"
    "v_mfma_f32_16x16x32_bf16 v[168:171], v[60:63], v[216:219], 0\n"
    "v_mfma_f32_16x16x32_bf16 v[172:175], v[60:63], v[220:223], 0\n"
    "v_mfma_f32_16x16x32_bf16 v[176:179], v[64:67], v[208:211], 0\n"
    "s_nop 3\n"
    "v_cvt_pk_bf16_f32 v224, v160, v161\n"
    "v_cvt_pk_bf16_f32 v225, v164, v165\n"
    "v_mfma_f32_16x16x32_bf16 v[180:183], v[64:67], v[212:215], 0\n"
    "v_cvt_pk_bf16_f32 v226, v168, v169\n"
    "v_cvt_pk_bf16_f32 v228, v162, v163\n"
    "v_cvt_pk_bf16_f32 v229, v166, v167\n"
    "v_cvt_pk_bf16_f32 v227, v172, v173\n"
    "ds_write_b128 v68, v[224:227] offset:0\n"
    "v_mfma_f32_16x16x32_bf16 v[184:187], v[64:67], v[216:219], 0\n"
    "v_cvt_pk_bf16_f32 v230, v170, v171\n"
    "v_cvt_pk_bf16_f32 v231, v174, v175\n"
    "v_mfma_f32_16x16x32_bf16 v[188:191], v[64:67], v[220:223], 0\n"
    "ds_write_b128 v68, v[228:231] offset:256\n"
    "v_cvt_pk_bf16_f32 v232, v176, v177\n"
    "v_cvt_pk_bf16_f32 v233, v180, v181\n"
    "v_cvt_pk_bf16_f32 v236, v178, v179\n"
    "v_cvt_pk_bf16_f32 v237, v182, v183\n"
    "v_cvt_pk_bf16_f32 v234, v184, v185\n"
    "v_cvt_pk_bf16_f32 v238, v186, v187\n"
    "s_nop 0\n"
    "v_cvt_pk_bf16_f32 v235, v188, v189\n"
    "ds_write_b128 v68, v[232:235] offset:2048\n"
    "v_cvt_pk_bf16_f32 v239, v190, v191\n"
    "ds_write_b128 v68, v[236:239] offset:2304\n"
    "s_waitcnt lgkmcnt(0)\n"
    "s_barrier\n"
    "ds_read_b128 v[192:195], v69\n"
    "ds_read_b128 v[196:199], v69 offset:4096\n"
    "ds_read_b128 v[200:203], v70\n"
    "ds_read_b128 v[204:207], v70 offset:4096\n"
    "s_waitcnt lgkmcnt(2)\n"
    "v_mov_b32 v208, v193\n"
    "v_mov_b32 v193, v196\n"
    "v_mov_b32 v196, v208\n"
    "v_mov_b32 v209, v195\n"
    "v_mfma_f32_16x16x16_bf16 v[160:163], v[56:57], v[192:193], 0\n"
    "v_mov_b32 v195, v198\n"
    "v_mov_b32 v198, v209\n"
    "s_waitcnt lgkmcnt(0)\n"
    "v_mfma_f32_16x16x16_bf16 v[164:167], v[56:57], v[196:197], 0\n"
    "v_mov_b32 v210, v201\n"
    "v_mov_b32 v201, v204\n"
    "v_mfma_f32_16x16x16_bf16 v[168:171], v[56:57], v[194:195], 0\n"
    "v_mov_b32 v204, v210\n"
    "v_mov_b32 v211, v203\n"
    "v_mfma_f32_16x16x16_bf16 v[172:175], v[56:57], v[198:199], 0\n"
    "v_mov_b32 v203, v206\n"
    "v_mov_b32 v206, v211\n"
    "v_mfma_f32_16x16x16_bf16 v[176:179], v[56:57], v[200:201], 0\n"
    "v_mfma_f32_16x16x16_bf16 v[180:183], v[56:57], v[204:205], 0\n"
    "v_cvt_pk_bf16_f32 v224, v160, v164\n"
    "v_cvt_pk_bf16_f32 v228, v161, v165\n"
    "v_mfma_f32_16x16x16_bf16 v[184:187], v[56:57], v[202:203], 0\n"
    "v_cvt_pk_bf16_f32 v232, v162, v166\n"
    "v_cvt_pk_bf16_f32 v236, v163, v167\n"
    "v_mfma_f32_16x16x16_bf16 v[188:191], v[56:57], v[206:207], 0\n"
    "v_cvt_pk_bf16_f32 v225, v168, v172\n"
    "v_cvt_pk_bf16_f32 v229, v169, v173\n"
    "v_cvt_pk_bf16_f32 v233, v170, v174\n"
    "v_cvt_pk_bf16_f32 v237, v171, v175\n"
    "v_cvt_pk_bf16_f32 v226, v176, v180\n"
    "v_cvt_pk_bf16_f32 v230, v177, v181\n"
    "v_cvt_pk_bf16_f32 v234, v178, v182\n"
    "v_cvt_pk_bf16_f32 v238, v179, v183\n"
    "v_cvt_pk_bf16_f32 v227, v184, v188\n"
    "buffer_store_dwordx4 v[224:227], v71, s[16:19], s60 offen sc1\n"
    "v_cvt_pk_bf16_f32 v231, v185, v189\n"
    "buffer_store_dwordx4 v[228:231], v71, s[16:19], s61 offen sc1\n"
    "v_cvt_pk_bf16_f32 v235, v186, v190\n"
    "s_waitcnt vmcnt(1)\n"
    "buffer_store_dwordx4 v[232:235], v71, s[16:19], s62 offen sc1\n"
    "v_cvt_pk_bf16_f32 v239, v187, v191\n"
    "s_waitcnt vmcnt(1)\n"
    "buffer_store_dwordx4 v[236:239], v71, s[16:19], s63 offen sc1\n"
    "s_endpgm\n"
    ".Lspa_%=:\n"
    "v_add_u32 v224, 32768, v248\n"
    "v_add_u32 v225, 32768, v249\n"
    "v_add_u32 v226, 34816, v248\n"
    "v_add_u32 v227, 34816, v249\n"
    "ds_read_u16_d16_hi v128, v226 offset:0\n"
    "ds_read_u16_d16_hi v129, v226 offset:512\n"
    "ds_read_u16_d16_hi v130, v227 offset:1024\n"
    "ds_read_u16_d16_hi v131, v227 offset:512\n"
    "ds_read_u16_d16_hi v144, v226 offset:1024\n"
    "ds_read_u16_d16_hi v145, v226 offset:1536\n"
    "ds_read_u16_d16_hi v146, v227 offset:2048\n"
    "ds_read_u16_d16_hi v147, v227 offset:1536\n"
    "ds_read_u16_d16_hi v132, v226 offset:64\n"
    "ds_read_u16_d16_hi v133, v226 offset:576\n"
    "ds_read_u16_d16_hi v134, v227 offset:960\n"
    "ds_read_u16_d16_hi v135, v227 offset:448\n"
    "ds_read_u16_d16_hi v148, v226 offset:1088\n"
    "ds_read_u16_d16_hi v149, v226 offset:1600\n"
    "ds_read_u16_d16_hi v150, v227 offset:1984\n"
    "ds_read_u16_d16_hi v151, v227 offset:1472\n"
    "ds_read_u16_d16_hi v136, v226 offset:2\n"
    "ds_read_u16_d16_hi v137, v226 offset:514\n"
    "ds_read_u16_d16_hi v138, v227 offset:1022\n"
    "ds_read_u16_d16_hi v139, v227 offset:510\n"
    "ds_read_u16_d16_hi v152, v226 offset:1026\n"
    "ds_read_u16_d16_hi v153, v226 offset:1538\n"
    "ds_read_u16_d16_hi v154, v227 offset:2046\n"
    "ds_read_u16_d16_hi v155, v227 offset:1534\n"
    "ds_read_u16_d16_hi v140, v226 offset:66\n"
    "ds_read_u16_d16_hi v141, v226 offset:578\n"
    "ds_read_u16_d16_hi v142, v227 offset:958\n"
    "ds_read_u16_d16_hi v143, v227 offset:446\n"
    "ds_read_u16_d16_hi v156, v226 offset:1090\n"
    "ds_read_u16_d16_hi v157, v226 offset:1602\n"
    "ds_read_u16_d16_hi v158, v227 offset:1982\n"
    "ds_read_u16_d16_hi v159, v227 offset:1470\n"
    "v_mov_b32 v192, 0\n"
    "v_mov_b32 v193, 0\n"
    "v_mov_b32 v194, 0\n"
    "v_mov_b32 v195, 0\n"
    "v_mov_b32 v196, 0\n"
    "v_mov_b32 v197, 0\n"
    "v_mov_b32 v198, 0\n"
    "v_mov_b32 v199, 0\n"
    "v_mov_b32 v200, 0\n"
    "v_mov_b32 v201, 0\n"
    "v_mov_b32 v202, 0\n"
    "v_mov_b32 v203, 0\n"
    "v_mov_b32 v204, 0\n"
    "v_mov_b32 v205, 0\n"
    "v_mov_b32 v206, 0\n"
    "v_mov_b32 v207, 0\n"
    "v_mov_b32 v208, 0\n"
    "v_mov_b32 v209, 0\n"
    "v_mov_b32 v210, 0\n"
    "v_mov_b32 v211, 0\n"
    "v_mov_b32 v212, 0\n"
    "v_mov_b32 v213, 0\n"
    "v_mov_b32 v214, 0\n"
    "v_mov_b32 v215, 0\n"
    "v_mov_b32 v216, 0\n"
    "v_mov_b32 v217, 0\n"
    "v_mov_b32 v218, 0\n"
    "v_mov_b32 v219, 0\n"
    "v_mov_b32 v220, 0\n"
    "v_mov_b32 v221, 0\n"
    "v_mov_b32 v222, 0\n"
    "v_mov_b32 v223, 0\n"
    "ds_read_u16_d16_hi v192, v224 offset:0\n"
    "ds_read_u16_d16_hi v193, v224 offset:512\n"
    "ds_read_u16_d16_hi v194, v225 offset:1024\n"
    "ds_read_u16_d16_hi v195, v225 offset:512\n"
    "ds_read_u16_d16_hi v208, v224 offset:1024\n"
    "ds_read_u16_d16_hi v209, v224 offset:1536\n"
    "ds_read_u16_d16_hi v210, v225 offset:2048\n"
    "ds_read_u16_d16_hi v211, v225 offset:1536\n"
    "ds_read_u16_d16_hi v196, v224 offset:64\n"
    "ds_read_u16_d16_hi v197, v224 offset:576\n"
    "ds_read_u16_d16_hi v198, v225 offset:960\n"
    "ds_read_u16_d16_hi v199, v225 offset:448\n"
    "ds_read_u16_d16_hi v212, v224 offset:1088\n"
    "ds_read_u16_d16_hi v213, v224 offset:1600\n"
    "ds_read_u16_d16_hi v214, v225 offset:1984\n"
    "ds_read_u16_d16_hi v215, v225 offset:1472\n"
    "ds_read_u16_d16_hi v200, v224 offset:2\n"
    "ds_read_u16_d16_hi v201, v224 offset:514\n"
    "ds_read_u16_d16_hi v202, v225 offset:1022\n"
    "ds_read_u16_d16_hi v203, v225 offset:510\n"
    "ds_read_u16_d16_hi v216, v224 offset:1026\n"
    "ds_read_u16_d16_hi v217, v224 offset:1538\n"
    "ds_read_u16_d16_hi v218, v225 offset:2046\n"
    "ds_read_u16_d16_hi v219, v225 offset:1534\n"
    "ds_read_u16_d16_hi v204, v224 offset:66\n"
    "ds_read_u16_d16_hi v205, v224 offset:578\n"
    "ds_read_u16_d16_hi v206, v225 offset:958\n"
    "ds_read_u16_d16_hi v207, v225 offset:446\n"
    "ds_read_u16_d16_hi v220, v224 offset:1090\n"
    "ds_read_u16_d16_hi v221, v224 offset:1602\n"
    "ds_read_u16_d16_hi v222, v225 offset:1982\n"
    "ds_read_u16_d16_hi v223, v225 offset:1470\n"
    "v_add_u32 v228, 110592, v15\n"
    "s_lshl_b32 s64, s64, 16\n"
    "s_lshl_b32 s65, s65, 16\n"
    "s_waitcnt lgkmcnt(0)\n"
    "v_writelane_b32 v194, s64, 0\n"
    "v_writelane_b32 v130, s65, 0\n"
    "v_sub_f32 v192, v192, v128\n"
    "v_mul_f32 v192, 0.5, v192\n"
    "v_sub_f32 v193, v193, v129\n"
    "v_mul_f32 v193, 0.5, v193\n"
    "v_sub_f32 v194, v194, v130\n"
    "v_mul_f32 v194, 0.5, v194\n"
    "v_sub_f32 v195, v195, v131\n"
    "v_mul_f32 v195, 0.5, v195\n"
    "ds_write_b128 v228, v[192:195] offset:0\n"
    "v_sub_f32 v196, v196, v132\n"
    "v_mul_f32 v196, 0.5, v196\n"
    "v_sub_f32 v197, v197, v133\n"
    "v_mul_f32 v197, 0.5, v197\n"
    "v_sub_f32 v198, v198, v134\n"
    "v_mul_f32 v198, 0.5, v198\n"
    "v_sub_f32 v199, v199, v135\n"
    "v_mul_f32 v199, 0.5, v199\n"
    "ds_write_b128 v228, v[196:199] offset:1024\n"
    "v_sub_f32 v200, v200, v136\n"
    "v_mul_f32 v200, 0.5, v200\n"
    "v_sub_f32 v201, v201, v137\n"
    "v_mul_f32 v201, 0.5, v201\n"
    "v_sub_f32 v202, v202, v138\n"
    "v_mul_f32 v202, 0.5, v202\n"
    "v_sub_f32 v203, v203, v139\n"
    "v_mul_f32 v203, 0.5, v203\n"
    "ds_write_b128 v228, v[200:203] offset:2048\n"
    "v_sub_f32 v204, v204, v140\n"
    "v_mul_f32 v204, 0.5, v204\n"
    "v_sub_f32 v205, v205, v141\n"
    "v_mul_f32 v205, 0.5, v205\n"
    "v_sub_f32 v206, v206, v142\n"
    "v_mul_f32 v206, 0.5, v206\n"
    "v_sub_f32 v207, v207, v143\n"
    "v_mul_f32 v207, 0.5, v207\n"
    "ds_write_b128 v228, v[204:207] offset:3072\n"
    "v_sub_f32 v208, v208, v144\n"
    "v_mul_f32 v208, 0.5, v208\n"
    "v_writelane_b32 v208, 0, 0\n"
    "v_sub_f32 v209, v209, v145\n"
    "v_mul_f32 v209, 0.5, v209\n"
    "v_sub_f32 v210, v210, v146\n"
    "v_mul_f32 v210, 0.5, v210\n"
    "v_writelane_b32 v210, 0, 0\n"
    "v_sub_f32 v211, v211, v147\n"
    "v_mul_f32 v211, 0.5, v211\n"
    "ds_write_b128 v228, v[208:211] offset:4096\n"
    "v_sub_f32 v212, v212, v148\n"
    "v_mul_f32 v212, 0.5, v212\n"
    "v_sub_f32 v213, v213, v149\n"
    "v_mul_f32 v213, 0.5, v213\n"
    "v_sub_f32 v214, v214, v150\n"
    "v_mul_f32 v214, 0.5, v214\n"
    "v_sub_f32 v215, v215, v151\n"
    "v_mul_f32 v215, 0.5, v215\n"
    "ds_write_b128 v228, v[212:215] offset:5120\n"
    "v_sub_f32 v216, v216, v152\n"
    "v_mul_f32 v216, 0.5, v216\n"
    "v_sub_f32 v217, v217, v153\n"
    "v_mul_f32 v217, 0.5, v217\n"
    "v_sub_f32 v218, v218, v154\n"
    "v_mul_f32 v218, 0.5, v218\n"
    "v_sub_f32 v219, v219, v155\n"
    "v_mul_f32 v219, 0.5, v219\n"
    "ds_write_b128 v228, v[216:219] offset:6144\n"
    "v_sub_f32 v220, v220, v156\n"
    "v_mul_f32 v220, 0.5, v220\n"
    "v_sub_f32 v221, v221, v157\n"
    "v_mul_f32 v221, 0.5, v221\n"
    "v_sub_f32 v222, v222, v158\n"
    "v_mul_f32 v222, 0.5, v222\n"
    "v_sub_f32 v223, v223, v159\n"
    "v_mul_f32 v223, 0.5, v223\n"
    "ds_write_b128 v228, v[220:223] offset:7168\n"
    "s_waitcnt lgkmcnt(0)\n"
    "s_branch .Lspa_ret_%=\n"
    ".Lspb_%=:\n"
    "v_add_u32 v115, 102400, v15\n"
    "ds_write_b128 v115, v[192:195] offset:0\n"
    "ds_write_b128 v115, v[196:199] offset:1024\n"
    "ds_write_b128 v115, v[200:203] offset:2048\n"
    "ds_write_b128 v115, v[204:207] offset:3072\n"
    "ds_write_b128 v115, v[208:211] offset:4096\n"
    "ds_write_b128 v115, v[212:215] offset:5120\n"
    "ds_write_b128 v115, v[216:219] offset:6144\n"
    "ds_write_b128 v115, v[220:223] offset:7168\n"
    "ds_read_b128 v[128:131], v115 offset:8192\n"
    "ds_read_b128 v[132:135], v115 offset:9216\n"
    "ds_read_b128 v[136:139], v115 offset:10240\n"
    "ds_read_b128 v[140:143], v115 offset:11264\n"
    "ds_read_b128 v[144:147], v115 offset:12288\n"
    "ds_read_b128 v[148:151], v115 offset:13312\n"
    "ds_read_b128 v[152:155], v115 offset:14336\n"
    "ds_read_b128 v[156:159], v115 offset:15360\n"
    "v_xor_b32 v114, 0x3f0, v115\n"
    "v_sub_u32 v116, 16, v4\n"
    "v_bfe_u32 v117, v116, 2, 2\n"
    "v_and_b32 v116, 3, v116\n"
    "v_lshl_or_b32 v116, v116, 2, v117\n"
    "v_xor_b32 v117, 3, v3\n"
    "v_lshl_or_b32 v116, v117, 4, v116\n"
    "v_lshlrev_b32 v116, 4, v116\n"
    "v_add_u32 v113, 102400, v116\n"
    "v_add_u32 v112, 104448, v116\n"
    "s_mov_b32 exec_lo, 0x10001\n"
    "s_mov_b32 exec_hi, 0x10001\n"
    "v_mov_b32 v113, v112\n"
    "v_add_u32 v116, 0x100, v116\n"
    "v_and_b32 v116, 0x3ff, v116\n"
    "v_add_u32 v112, 102400, v116\n"
    "s_mov_b64 exec, -1\n"
    "ds_read_b128 v[16:19], v112 offset:0\n"
    "ds_read_b128 v[20:23], v112 offset:1024\n"
    "ds_read_b128 v[24:27], v113 offset:0\n"
    "ds_read_b128 v[28:31], v113 offset:1024\n"
    "ds_read_b128 v[224:227], v114 offset:6144\n"
    "ds_read_b128 v[228:231], v114 offset:7168\n"
    "ds_read_b128 v[232:235], v114 offset:4096\n"
    "ds_read_b128 v[236:239], v114 offset:5120\n"
    "s_waitcnt lgkmcnt(0)\n"
    "s_mov_b64 exec, 1\n"
    "v_mov_b32 v116, v16\n"
    "v_mov_b32 v16, v17\n"
    "v_mov_b32 v17, v18\n"
    "v_mov_b32 v18, v19\n"
    "v_mov_b32 v19, v116\n"
    "v_mov_b32 v116, v20\n"
    "v_mov_b32 v20, v21\n"
    "v_mov_b32 v21, v22\n"
    "v_mov_b32 v22, v23\n"
    "v_mov_b32 v23, v116\n"
    "s_mov_b64 exec, -1\n"
    "v_sub_f32 v192, v19, v192\n"
    "v_add_f32 v196, v23, v196\n"
    "v_fma_f32 v160, -v196, v128, v160\n"
    "v_fmac_f32 v164, v192, v128\n"
    "v_fma_f32 v160, v192, v144, v160\n"
    "v_fma_f32 v164, v196, v144, v164\n"
    "v_sub_f32 v193, v18, v193\n"
    "v_add_f32 v197, v22, v197\n"
    "v_fma_f32 v161, -v197, v129, v161\n"
    "v_fmac_f32 v165, v193, v129\n"
    "v_fma_f32 v161, v193, v145, v161\n"
    "v_fma_f32 v165, v197, v145, v165\n"
    "v_sub_f32 v194, v17, v194\n"
    "v_add_f32 v198, v21, v198\n"
    "v_fma_f32 v162, -v198, v130, v162\n"
    "v_fmac_f32 v166, v194, v130\n"
    "v_fma_f32 v162, -v194, v146, v162\n"
    "v_fma_f32 v166, -v198, v146, v166\n"
    "v_sub_f32 v195, v16, v195\n"
    "v_add_f32 v199, v20, v199\n"
    "v_fma_f32 v163, -v199, v131, v163\n"
    "v_fmac_f32 v167, v195, v131\n"
    "v_fma_f32 v163, -v195, v147, v163\n"
    "v_fma_f32 v167, -v199, v147, v167\n"
    "v_sub_f32 v200, v27, v200\n"
    "v_add_f32 v204, v31, v204\n"
    "v_fma_f32 v168, -v204, v132, v168\n"
    "v_fmac_f32 v172, v200, v132\n"
    "v_fma_f32 v168, v200, v148, v168\n"
    "v_fma_f32 v172, v204, v148, v172\n"
    "v_sub_f32 v201, v26, v201\n"
    "v_add_f32 v205, v30, v205\n"
    "v_fma_f32 v169, -v205, v133, v169\n"
    "v_fmac_f32 v173, v201, v133\n"
    "v_fma_f32 v169, v201, v149, v169\n"
    "v_fma_f32 v173, v205, v149, v173\n"
    "v_sub_f32 v202, v25, v202\n"
    "v_add_f32 v206, v29, v206\n"
    "v_fma_f32 v170, -v206, v134, v170\n"
    "v_fmac_f32 v174, v202, v134\n"
    "v_fma_f32 v170, -v202, v150, v170\n"
    "v_fma_f32 v174, -v206, v150, v174\n"
    "v_sub_f32 v203, v24, v203\n"
    "v_add_f32 v207, v28, v207\n"
    "v_fma_f32 v171, -v207, v135, v171\n"
    "v_fmac_f32 v175, v203, v135\n"
    "v_fma_f32 v171, -v203, v151, v171\n"
    "v_fma_f32 v175, -v207, v151, v175\n"
    "v_sub_f32 v208, v227, v208\n"
    "v_add_f32 v212, v231, v212\n"
    "v_fma_f32 v176, -v212, v136, v176\n"
    "v_fmac_f32 v180, v208, v136\n"
    "v_fma_f32 v176, v208, v152, v176\n"
    "v_fma_f32 v180, v212, v152, v180\n"
    "v_sub_f32 v209, v226, v209\n"
    "v_add_f32 v213, v230, v213\n"
    "v_fma_f32 v177, -v213, v137, v177\n"
    "v_fmac_f32 v181, v209, v137\n"
    "v_fma_f32 v177, v209, v153, v177\n"
    "v_fma_f32 v181, v213, v153, v181\n"
    "v_sub_f32 v210, v225, v210\n"
    "v_add_f32 v214, v229, v214\n"
    "v_fma_f32 v178, -v214, v138, v178\n"
    "v_fmac_f32 v182, v210, v138\n"
    "v_fma_f32 v178, -v210, v154, v178\n"
    "v_fma_f32 v182, -v214, v154, v182\n"
    "v_sub_f32 v211, v224, v211\n"
    "v_add_f32 v215, v228, v215\n"
    "v_fma_f32 v179, -v215, v139, v179\n"
    "v_fmac_f32 v183, v211, v139\n"
    "v_fma_f32 v179, -v211, v155, v179\n"
    "v_fma_f32 v183, -v215, v155, v183\n"
    "v_sub_f32 v216, v235, v216\n"
    "v_add_f32 v220, v239, v220\n"
    "v_fma_f32 v184, -v220, v140, v184\n"
    "v_fmac_f32 v188, v216, v140\n"
    "v_fma_f32 v184, v216, v156, v184\n"
    "v_fma_f32 v188, v220, v156, v188\n"
    "v_sub_f32 v217, v234, v217\n"
    "v_add_f32 v221, v238, v221\n"
    "v_fma_f32 v185, -v221, v141, v185\n"
    "v_fmac_f32 v189, v217, v141\n"
    "v_fma_f32 v185, v217, v157, v185\n"
    "v_fma_f32 v189, v221, v157, v189\n"
    "v_sub_f32 v218, v233, v218\n"
    "v_add_f32 v222, v237, v222\n"
    "v_fma_f32 v186, -v222, v142, v186\n"
    "v_fmac_f32 v190, v218, v142\n"
    "v_fma_f32 v186, -v218, v158, v186\n"
    "v_fma_f32 v190, -v222, v158, v190\n"
    "v_sub_f32 v219, v232, v219\n"
    "v_add_f32 v223, v236, v223\n"
    "v_fma_f32 v187, -v223, v143, v187\n"
    "v_fmac_f32 v191, v219, v143\n"
    "v_fma_f32 v187, -v219, v159, v187\n"
    "v_fma_f32 v191, -v223, v159, v191\n"
    "s_branch .Lspb_ret_%=\n"
    ".Lend_%=:\n"
    :
    : "{s[0:1]}"(__builtin_amdgcn_kernarg_segment_ptr()), "{s2}"(__builtin_amdgcn_workgroup_id_x()),
      "{v0}"(__builtin_amdgcn_workitem_id_x()), "i"((__attribute__((address_space(3))) unsigned char*)lds)
    : "v1", "v255", "s3", "s69", "vcc", "memory");
}

extern "C" void kernel_launch(void* const* d_in, const int* in_sizes, int n_in, void* d_out, int out_size, void* d_ws, size_t ws_size,
                              hipStream_t stream) {
  char* ws = (char*)d_ws;
  Params P = {ws, d_in[0], d_out, d_in[3], d_in[1], d_in[4], d_in[2], d_in[5]};
  gfnet_fused<<<256, 512, 0, stream>>>(P);
}
```
